```python
import torch

def _softmax(x, axis=-1):
    return torch.softmax(x, dim=axis)
def _gelu(x):
    return torch.nn.functional.gelu(x, approximate="tanh")
def _silu(x):
    return torch.nn.functional.silu(x)
def _sigmoid(x):
    return torch.sigmoid(x)
def _layernorm(x, axis=-1, eps=1e-5):
    m = x.mean(dim=axis, keepdim=True)
    v = x.var(dim=axis, keepdim=True, unbiased=False)
    return (x - m) / torch.sqrt(v + eps)
def _scatter_add(tgt, idx, src, axis=0):
    return tgt.clone().index_add_(axis, idx, src)
def _segment_sum(src, seg, n):
    out = torch.zeros((n,) + src.shape[1:], dtype=src.dtype)
    return out.index_add_(0, seg, src)
def _pad(x, pw):
    flat = tuple(v for pair in reversed(pw) for v in pair)
    return torch.nn.functional.pad(x, flat)

kernel_name = 'concat_heads__mha_block_cached__gqa_block_cached__near_ridge'

def setup_inputs():

    _w = (lambda a: (lambda t: t.to(torch.bfloat16) if t.is_floating_point() else t)(torch.as_tensor(a)))
    def _A_setup():
        B, Sq, D = 16, 112, 512
        H, Sk, Dh = 32, 512, 128
        return {
            "x": _w((lambda *s: torch.randn(*s, dtype=torch.bfloat16) * 0.8)(B, Sq, D)),
            "k_cache": _w((lambda *s: torch.randn(*s, dtype=torch.bfloat16) * 0.8)(B, H, Sk, Dh)),
            "v_cache": _w((lambda *s: torch.randn(*s, dtype=torch.bfloat16) * 0.05)(B, H, Sk, Dh)),
            "wq": _w((lambda *s: torch.randn(*s, dtype=torch.bfloat16) * 0.8)(D, H * Dh) * (D ** -0.5)),
            "wo": _w((lambda *s: torch.randn(*s, dtype=torch.bfloat16) * 0.05)(H * Dh, D) * ((H * Dh) ** -0.5)),
        }
    _A_in = _A_setup()

    def _B_setup():
        B, Sq, D = 16, 112, 512
        Hq, Hkv, Sk, Dh = 32, 32, 512, 128
        assert Hq % Hkv == 0, (Hq, Hkv)
        return {
            "x": _w((lambda *s: torch.randn(*s, dtype=torch.bfloat16) * 0.8)(B, Sq, D)),
            "k_cache": _w((lambda *s: torch.randn(*s, dtype=torch.bfloat16) * 0.8)(B, Hkv, Sk, Dh)),
            "v_cache": _w((lambda *s: torch.randn(*s, dtype=torch.bfloat16) * 0.05)(B, Hkv, Sk, Dh)),
            "wq": _w((lambda *s: torch.randn(*s, dtype=torch.bfloat16) * 0.8)(D, Hq * Dh) * (D ** -0.5)),
            "wo": _w((lambda *s: torch.randn(*s, dtype=torch.bfloat16) * 0.05)(Hq * Dh, D) * ((Hq * Dh) ** -0.5)),
        }
    _B_in = _B_setup()

    return {
        "A_x": _A_in["x"],
        "A_k_cache": _A_in["k_cache"],
        "A_v_cache": _A_in["v_cache"],
        "A_wq": _A_in["wq"],
        "A_wo": _A_in["wo"],
        "B_x": _B_in["x"],
        "B_k_cache": _B_in["k_cache"],
        "B_v_cache": _B_in["v_cache"],
        "B_wq": _B_in["wq"],
        "B_wo": _B_in["wo"],
    }

def reference(A_x, A_k_cache, A_v_cache, A_wq, A_wo, B_x, B_k_cache, B_v_cache, B_wq, B_wo):
    def _A_ref(x, k_cache, v_cache, wq, wo):
        B, Sq, D = x.shape
        H, Dh = 32, 128
        q = (lambda x, *d: x.permute(*d))((x @ wq).reshape(B, Sq, H, Dh), 0, 2, 1, 3)
        scores = torch.einsum("bhqd,bhkd->bhqk", q, k_cache) * (Dh ** -0.5)
        p = _softmax(scores, axis=-1)
        o = torch.einsum("bhqk,bhkd->bhqd", p, v_cache)
        return (lambda x, *d: x.permute(*d))(o, 0, 2, 1, 3).reshape(B, Sq, H * Dh) @ wo

    def _B_ref(x, k_cache, v_cache, wq, wo):
        B, Sq, D = x.shape
        Hq, Dh = 32, 128
        Hkv = k_cache.shape[1]
        rep = Hq // Hkv
        q = (lambda x, *d: x.permute(*d))((x @ wq).reshape(B, Sq, Hq, Dh), 0, 2, 1, 3)
        k = (lambda x, n, axis: torch.repeat_interleave(x, n, dim=axis))(k_cache, rep, axis=1)
        v = (lambda x, n, axis: torch.repeat_interleave(x, n, dim=axis))(v_cache, rep, axis=1)
        scores = torch.einsum("bhqd,bhkd->bhqk", q, k) * (Dh ** -0.5)
        p = _softmax(scores, axis=-1)
        o = torch.einsum("bhqk,bhkd->bhqd", p, v)
        return (lambda x, *d: x.permute(*d))(o, 0, 2, 1, 3).reshape(B, Sq, Hq * Dh) @ wo

    a = _A_ref(A_x, A_k_cache, A_v_cache, A_wq, A_wo)
    b = _B_ref(B_x, B_k_cache, B_v_cache, B_wq, B_wo)
    return torch.concatenate([a, b], axis=-1)
```

```cpp
#define DMAPOS 2
#define APOS -1
#define WDEF
#include <hip/hip_runtime.h>
#include <hip/hip_bf16.h>

typedef unsigned u32;
typedef unsigned long long u64;
typedef __bf16 bf16x8 __attribute__((ext_vector_type(8)));
typedef __bf16 bf16x4 __attribute__((ext_vector_type(4)));
typedef __bf16 bf16x2 __attribute__((ext_vector_type(2)));
typedef float f32x16 __attribute__((ext_vector_type(16)));
typedef float f32x2 __attribute__((ext_vector_type(2)));
typedef u32 u32x4 __attribute__((ext_vector_type(4)));
#define LAS __attribute__((address_space(3)))
#define DI __device__ __forceinline__

constexpr int NB = 16, SQ = 112, DM = 512, NH = 32, SK = 512, DH = 128;
constexpr float QSCALE = 0.12751743082f;
constexpr float THR = 8.0f;

constexpr u32 SLOT = 16384;
constexpr u32 L_K = 0;
constexpr u32 L_V = 3 * SLOT;
constexpr u32 L_W = 6 * SLOT;
constexpr u32 L_QH = 8 * SLOT;
constexpr u32 LDS_BYTES = 10 * SLOT;

constexpr size_t WS_P = 0;

struct Params {
  const void* x[2]; const void* k[2]; const void* v[2]; const void* wq[2]; const void* wo[2];
  void* out; void* ws;
};

DI u32 rfl(u32 x) { return __builtin_amdgcn_readfirstlane(x); }
DI __amdgpu_buffer_rsrc_t mksrd(const void* p, u32 bytes) { return __builtin_amdgcn_make_buffer_rsrc((void*)p, (short)0, (int)bytes, 0x00020000); }
DI void dma_nt(u32 m0v, u32 voff, __amdgpu_buffer_rsrc_t srd, u32 soff) {
  asm volatile("s_mov_b32 m0, %0\n\ts_nop 0\n\tbuffer_load_dwordx4 %1, %2, %3 offen nt lds" :: "s"(m0v), "v"(voff), "s"(srd), "s"(soff) : "memory");
}
#define WPOL0 ""
DI void dma_w(u32 m0v, u32 voff, __amdgpu_buffer_rsrc_t srd, u32 soff) {
  asm volatile("s_mov_b32 m0, %0\n\ts_nop 0\n\tbuffer_load_dwordx4 %1, %2, %3 offen" WPOL0 " lds" :: "s"(m0v), "v"(voff), "s"(srd), "s"(soff) : "memory");
}
#define TROTM 0
#define TROT (TROTM == 1 ? (b & 7) : TROTM == 2 ? ((bid * 5) & 7) : TROTM == 3 ? ((b + xcd) & 7) : TROTM == 4 ? ((j + (xcd >> 1) * 2) & 7) : 0)
#define HROTM 0
#define HROT (HROTM == 1 ? (b & 3) : HROTM == 2 ? ((b >> 2) & 3) : 0)
#define KROTM 1
#define KROT (KROTM == 1 ? (((b >> 2) + 2 * (j & 1)) & 3) : 0)
#define DROT (KROTM == 1 ? ((b >> 1) & 1) : 0)

template <int N> DI void waitvm() { asm volatile("s_waitcnt vmcnt(%0)" :: "i"(N) : "memory"); }
DI void barrier_lgkm() { asm volatile("s_waitcnt lgkmcnt(0)\n\ts_barrier" ::: "memory"); }
template <int N> DI void barrier_vm() { asm volatile("s_waitcnt vmcnt(%0) lgkmcnt(0)\n\ts_barrier" :: "i"(N) : "memory"); }
DI u32 cvtpk(float lo, float hi) { f32x2 v = {lo, hi}; bf16x2 b = __builtin_convertvector(v, bf16x2); return __builtin_bit_cast(u32, b); }
DI bf16x8 pack8(const f32x16& a, int base, float s) {
  u32x4 w;
  w[0] = cvtpk(a[base + 0] * s, a[base + 1] * s); w[1] = cvtpk(a[base + 2] * s, a[base + 3] * s);
  w[2] = cvtpk(a[base + 4] * s, a[base + 5] * s); w[3] = cvtpk(a[base + 6] * s, a[base + 7] * s);
  return __builtin_bit_cast(bf16x8, w);
}
DI u32x4 pack8n(const f32x16& a, int base) {
  u32x4 w;
  w[0] = cvtpk(a[base + 0], a[base + 1]); w[1] = cvtpk(a[base + 2], a[base + 3]);
  w[2] = cvtpk(a[base + 4], a[base + 5]); w[3] = cvtpk(a[base + 6], a[base + 7]);
  return w;
}
DI bf16x8 lds_b128(LAS char* l, u32 off) { return *(LAS bf16x8*)(l + off); }
DI void lds_w128(LAS char* l, u32 off, bf16x8 v) { *(LAS bf16x8*)(l + off) = v; }
DI bf16x8 lds_tr2(LAS char* l, u32 off0, u32 off1) {
  bf16x4 a = __builtin_amdgcn_ds_read_tr16_b64_v4bf16((LAS bf16x4*)(l + off0));
  bf16x4 b = __builtin_amdgcn_ds_read_tr16_b64_v4bf16((LAS bf16x4*)(l + off1));
  return __builtin_shufflevector(a, b, 0, 1, 2, 3, 4, 5, 6, 7);
}
#define STAMP(i)
#define STAMPC(i)
#define SB() __builtin_amdgcn_sched_barrier(0)
#define TICK(i)
#define TICKDECL
#define TICKOUT
#define PIN(x) asm volatile("" : "+v"(x))
#define OPAQUE_S(x) asm volatile("" : "+s"(x))
DI float vmax3(float a, float b, float c) { return __builtin_elementwise_maximum(__builtin_elementwise_maximum(a, b), c); }
#define MFMA(a, b, c) __builtin_amdgcn_mfma_f32_32x32x16_bf16((a), (b), (c), 0, 0, 0)
#define EXP2(x) __builtin_amdgcn_exp2f(x)

__global__ __launch_bounds__(512, 2) void fused_attn(Params p) {
  extern __shared__ __attribute__((aligned(16))) char smem[];
  LAS char* L = (LAS char*)smem;
  const u32 ldsb = (u32)(size_t)smem;
  const int tid = threadIdx.x, lane = tid & 63;
  const u32 wave = rfl(tid >> 6);
  const u32 w = wave & 3;
  const u32 bid = blockIdx.x, xcd = bid & 7, j = bid >> 3;
  const u32 br = xcd & 1, hg = 2 * (xcd >> 1) + (j & 1), b = j >> 1;
  const u32 trot = TROT;
  const u32 hrot = HROT;
  const size_t kvoff = (size_t)((b * NH + hg * 4) * SK) * DH * 2;
  const __amdgpu_buffer_rsrc_t srdK = mksrd((const char*)p.k[br] + kvoff, 4 * SK * DH * 2);
  const __amdgpu_buffer_rsrc_t srdV = mksrd((const char*)p.v[br] + kvoff, 4 * SK * DH * 2);
#define LANE_CONSTS \
  int ln; asm volatile("v_mbcnt_lo_u32_b32 %0, -1, 0\n\tv_mbcnt_hi_u32_b32 %0, -1, %0" : "=v"(ln)); \
  const u32 cpart = 64u * (ln >> 5) + 16u * ((ln & 3) ^ (2u * (w & 1) + ((ln >> 4) & 1))); \
  const int r = ln & 31, h = ln >> 5, blk = (ln >> 4) & 1, q4 = (ln & 15) >> 2, p4 = ln & 3; \
  (void)cpart; (void)r; (void)h; (void)blk; (void)q4; (void)p4;
  auto kv_one = [&](const __amdgpu_buffer_rsrc_t srd, u32 st, u32 la, const int i, u32 vo) {
    const u32 tile = (((st >> 3) + hrot) & 3) * 8 + ((st + trot) & 7);
    const u32 lb = ldsb + la + 2048u * w, so = tile * SLOT + 2048u * w;
    dma_nt(rfl(lb + 1024u * (i & 1) + 8192u * (i >> 1)), vo, srd, rfl(so + 128u * (i & 1) + 8192u * (i >> 1)));
  };
  auto issue_kv = [&](const __amdgpu_buffer_rsrc_t srd, u32 st, u32 la, u32 vo) {
    #pragma unroll
    for (int i = 0; i < 4; ++i) kv_one(srd, st, la, i, vo);
  };

  STAMP(0); STAMPC(8);
  if (wave < 4) {
    LANE_CONSTS
    const u32 vo_kv = 256u * ((ln >> 2) & 7) + cpart;
    const u32 kA0 = 2048u * (r >> 3) + 64u * (r & 7) + 16u * ((u32)h ^ ((r >> 2) & 3));
    const u32 kA1 = 2048u * (r >> 3) + 64u * (r & 7) + 16u * ((u32)(2 + h) ^ ((r >> 2) & 3));
    const u32 vB0 = 64u * (4 * h + q4) + 16u * ((u32)(2 * blk + (p4 >> 1)) ^ (u32)h) + 8u * (p4 & 1);
    const u32 vB1 = 2048u + 64u * (4 * h + q4) + 16u * ((u32)(2 * blk + (p4 >> 1)) ^ (u32)(2 + h)) + 8u * (p4 & 1);
    const u32 qh = L_QH + w * 8192u + ln * 16u;
    const __amdgpu_buffer_rsrc_t srdX = mksrd((const char*)p.x[br] + (size_t)b * SQ * DM * 2, SQ * DM * 2);
    const u32 vo_xd = 1024u * ((ln >> 2) & 7) + cpart;
    const u32 rot = KROT;
    auto issue_x = [&](u32 jq, u32 la0, u32 la1) {
      const u32 kq = (jq + rot) & 3;
      const u32 so = 8192u * w + 256u * kq;
      const u32 l0 = ldsb + la0 + 2048u * w, l1 = ldsb + la1 + 2048u * w;
      dma_nt(rfl(l0), vo_xd, srdX, rfl(so));                 dma_nt(rfl(l0 + 1024u), vo_xd, srdX, rfl(so + 128u));
      dma_nt(rfl(l0 + 8192u), vo_xd, srdX, rfl(so + 32768u)); dma_nt(rfl(l0 + 9216u), vo_xd, srdX, rfl(so + 32896u));
      dma_nt(rfl(l1), vo_xd, srdX, rfl(so + 65536u));         dma_nt(rfl(l1 + 1024u), vo_xd, srdX, rfl(so + 65664u));
      dma_nt(rfl(l1 + 8192u), vo_xd, srdX, rfl(so + 98304u)); dma_nt(rfl(l1 + 9216u), vo_xd, srdX, rfl(so + 98432u));
    };
    issue_x(0, 2 * SLOT, 3 * SLOT); issue_x(1, 4 * SLOT, 5 * SLOT); issue_x(2, L_QH, L_QH + SLOT);
    barrier_vm<16>();
    barrier_vm<8>();
    issue_x(3, 2 * SLOT, 3 * SLOT);
    barrier_vm<8>();
    issue_kv(srdK, 0, L_K, vo_kv); issue_kv(srdK, 1, L_K + SLOT, vo_kv);
    barrier_vm<8>();
    barrier_lgkm();
    issue_kv(srdK, 2, L_K + 2 * SLOT, vo_kv);
    barrier_lgkm(); barrier_lgkm();
    barrier_vm<8>();
    bf16x8 kf[16];
    #pragma unroll
    for (int i = 0; i < 16; ++i) kf[i] = lds_b128(L, L_K + ((i & 1) ? kA1 : kA0) + 512u * ((i & 7) >> 1) + 8192u * (i >> 3));
    barrier_vm<4>();
    STAMP(1);
    bf16x8 qf[8], oa0[8];
    #pragma unroll
    for (int ks = 0; ks < 8; ++ks) { qf[ks] = lds_b128(L, qh + ks * 1024u); oa0[ks] = qf[ks]; }
    u32 s3 = 0;
    TICKDECL
    #pragma unroll 1
    for (int rd = 0; rd < 4; ++rd) {
      f32x16 O[4];
      #pragma unroll
      for (int d = 0; d < 4; ++d) O[d] = f32x16{};
      float m = 0.f, l = 0.f;
      f32x16 negm = f32x16{};
      #pragma unroll 1
      for (int t = 0; t < 8; ++t) {
        const u32 s3n = (s3 == 2) ? 0 : s3 + 1;
        const u32 vbase = L_V + s3 * SLOT, kbase = L_K + s3n * SLOT;
        const u32 sK = 8u * rd + t + 3, kdst = L_K + s3 * SLOT;
        const bool kdma = sK < 32;
        const u32 b0 = vbase + vB0, b1 = vbase + vB1, a0 = kbase + kA0, a1 = kbase + kA1;
        auto vread = [&](const int i) { return lds_tr2(L, b0 + 4096u * (i >> 2) + 512u * (i & 3), b1 + 4096u * (i >> 2) + 512u * (i & 3)); };
        auto kread = [&](const int i) { return lds_b128(L, ((i & 1) ? a1 : a0) + 512u * ((i & 7) >> 1) + 8192u * (i >> 3)); };
        bf16x8 vf[16];
        TICK(0);
        f32x16 S0 = MFMA(kf[0], qf[0], negm);
        #pragma unroll
        for (int ks = 1; ks < 8; ++ks) {
          S0 = MFMA(kf[ks], qf[ks], S0);
          if (APOS < 0 && (ks & 1) && kdma) { SB(); kv_one(srdK, sK, kdst, ks >> 1, vo_kv); }
        }
        SB();
        f32x16 S1 = MFMA(kf[8], qf[0], negm);
        float pa, pb;
        #pragma unroll
        for (int ks = 1; ks < 8; ++ks) {
          S1 = MFMA(kf[8 + ks], qf[ks], S1);
          vf[ks - 1] = vread(ks - 1);
          if (ks == 2) { pa = vmax3(S0[0], S0[1], S0[2]); pb = vmax3(S0[3], S0[4], S0[5]); }
          if (ks == 3) { pa = vmax3(pa, S0[6], S0[7]); pb = vmax3(pb, S0[8], S0[9]); }
          if (ks == 4) { pa = vmax3(pa, S0[10], S0[11]); pb = vmax3(pb, S0[12], S0[13]); }
          if (ks == 5) { pa = vmax3(pa, S0[14], S0[15]); }
          SB();
        }
        vf[7] = vread(7);
        asm volatile("" ::: "memory");
        SB();
        TICK(1);
        {
          float pc = vmax3(S1[0], S1[1], S1[2]), pd = vmax3(S1[3], S1[4], S1[5]);
          pa = vmax3(pa, S1[6], S1[7]); pb = vmax3(pb, S1[8], S1[9]);
          pc = vmax3(pc, S1[10], S1[11]); pd = vmax3(pd, S1[12], S1[13]);
          pa = vmax3(pa, S1[14], S1[15]);
          pa = vmax3(pa, pb, pc); pa = vmax3(pa, pd, pd);
        }
        if (__builtin_expect(t == 0 || __any(pa > THR), 0)) {
          auto sw = __builtin_amdgcn_permlane32_swap(__float_as_uint(pa), __float_as_uint(pa), false, false);
          const float pmx = fmaxf(__uint_as_float(sw[0]), __uint_as_float(sw[1]));
          const float delta = (t == 0) ? pmx : fmaxf(pmx, 0.f);
          const float alpha = (t == 0) ? 0.f : EXP2(-delta);
          m += delta; l *= alpha;
          #pragma unroll
          for (int i = 0; i < 16; ++i) { S0[i] -= delta; S1[i] -= delta; negm[i] = -m; }
          #pragma unroll
          for (int d = 0; d < 4; ++d)
            #pragma unroll
            for (int i = 0; i < 16; ++i) O[d][i] *= alpha;
        }
        SB();
        float ls0 = 0.f, ls1 = 0.f;
        u32x4 pw[4];
        #pragma unroll
        for (int i = 0; i < 4; ++i) {
          S0[2 * i] = EXP2(S0[2 * i]); S0[2 * i + 1] = EXP2(S0[2 * i + 1]);
          vf[8 + i] = vread(8 + i);
          ls0 += S0[2 * i]; PIN(ls0); ls1 += S0[2 * i + 1]; PIN(ls1);
          pw[0][i] = cvtpk(S0[2 * i], S0[2 * i + 1]);
          SB();
        }
        TICK(2);
        #pragma unroll
        for (int g = 0; g < 4; ++g) {
          #pragma unroll
          for (int d = 0; d < 4; ++d) {
            O[d] = MFMA(vf[4 * g + d], __builtin_bit_cast(bf16x8, pw[g]), O[d]);
            if (g == 0) vf[12 + d] = vread(12 + d);
            if (g < 3) {
              const int e = 2 * d + ((g + 1) & 1) * 8;
              if (g == 0) { S0[e] = EXP2(S0[e]); S0[e + 1] = EXP2(S0[e + 1]); ls0 += S0[e]; PIN(ls0); ls1 += S0[e + 1]; PIN(ls1); pw[1][d] = cvtpk(S0[e], S0[e + 1]); }
              else        { S1[e] = EXP2(S1[e]); S1[e + 1] = EXP2(S1[e + 1]); ls0 += S1[e]; PIN(ls0); ls1 += S1[e + 1]; PIN(ls1); pw[g + 1][d] = cvtpk(S1[e], S1[e + 1]); }
            }
            SB();
            if (g > 0) kf[4 * (g - 1) + d] = kread(4 * (g - 1) + d);
            if (g == 3) kf[12 + d] = kread(12 + d);
            if (g == APOS && kdma) kv_one(srdK, sK, kdst, d, vo_kv);
            SB();
          }
        }
        l += ls0 + ls1;
        s3 = s3n;
        TICK(3);
        if (kdma) barrier_vm<4>(); else barrier_vm<0>();
        TICK(4);
      }
      auto sw = __builtin_amdgcn_permlane32_swap(__float_as_uint(l), __float_as_uint(l), false, false);
      const float inv = 1.0f / (__uint_as_float(sw[0]) + __uint_as_float(sw[1]));
      bf16x8 opk[8];
      #pragma unroll
      for (int d = 0; d < 4; ++d) { opk[2 * d] = pack8(O[d], 0, inv); opk[2 * d + 1] = pack8(O[d], 8, inv); }
      if (rd < 3) {
        #pragma unroll
        for (int ks = 0; ks < 8; ++ks) qf[ks] = lds_b128(L, qh + ks * 1024u);
        if (rd == 0) {
          #pragma unroll
          for (int ks = 0; ks < 8; ++ks) oa0[ks] = opk[ks];
        } else {
          #pragma unroll
          for (int ks = 0; ks < 8; ++ks) lds_w128(L, qh + ks * 1024u, opk[ks]);
        }
      } else {
        #pragma unroll
        for (int ks = 0; ks < 8; ++ks) {
          lds_w128(L, 0 * 32768u + w * 8192u + ks * 1024u + ln * 16u, oa0[ks]);
          lds_w128(L, 3 * 32768u + w * 8192u + ks * 1024u + ln * 16u, opk[ks]);
        }
      }
      STAMP(2 + rd);
    }
    TICKOUT
  } else {
    LANE_CONSTS
    const u32 vo_kv = 256u * ((ln >> 2) & 7) + cpart;
    const __amdgpu_buffer_rsrc_t srdW = mksrd(p.wq[br], DM * NH * DH * 2);
    const u32 rot = KROT, dh0 = DROT;
    const u32 vo_w = 8192u * ((ln >> 2) & 7) + cpart;
    auto w_one = [&](u32 rdn, u32 pi, u32 la, const int g) {
      const u32 hd = hg * 4 + ((rdn + hrot) & 3);
      const u32 kq = ((pi & 3) + rot) & 3, dh = (pi >> 2) ^ dh0;
      const u32 so = (128u * kq + 8u * w) * 8192u + (128u * hd + 64u * dh) * 2u;
      dma_w(rfl(ldsb + la + 1024u * w + 4096u * g), vo_w, srdW, rfl(so + 32u * 8192u * g));
    };
    auto issue_w = [&](u32 rdn, u32 pi, u32 la) {
      #pragma unroll
      for (int g = 0; g < 4; ++g) w_one(rdn, pi, la, g);
    };
    bf16x8 xf[32];
    const u32 xA0 = 8192u * (w & 1) + 2048u * (r >> 3) + 64u * (r & 7) + 16u * ((u32)h ^ ((r >> 2) & 3));
    const u32 xA1 = 8192u * (w & 1) + 2048u * (r >> 3) + 64u * (r & 7) + 16u * ((u32)(2 + h) ^ ((r >> 2) & 3));
    auto read_x = [&](const int jq, u32 la0, u32 la1) {
      u32 la = (w & 2) ? la1 : la0;
      OPAQUE_S(la);
      #pragma unroll
      for (int i = 0; i < 8; ++i) xf[8 * jq + i] = lds_b128(L, la + ((i & 1) ? xA1 : xA0) + 512u * (i >> 1));
    };
    const u32 wB0 = 1024u * h + 64u * q4 + 16u * ((u32)(2 * blk + (p4 & 1)) ^ (u32)(2 * h)) + 8u * (p4 >> 1);
    const u32 wB1 = 1024u * h + 256u + 64u * q4 + 16u * ((u32)(2 * blk + (p4 & 1)) ^ (u32)(2 * h + 1)) + 8u * (p4 >> 1);
    const u32 qh = L_QH + w * 8192u + ln * 16u;
    f32x16 qa[2];
    auto piece = [&](const int pi, u32 la, auto&& dma) {
      for (int jj = 0; jj < 8; ++jj) dma(jj);
      OPAQUE_S(la);
      const u32 b0 = la + wB0, b1 = la + wB1;
      bf16x8 fr[8][2];
      #pragma unroll
      for (int ks = 0; ks < 2; ++ks)
        #pragma unroll
        for (int d = 0; d < 2; ++d) fr[ks][d] = lds_tr2(L, b0 + 2048u * ks + 512u * d, b1 + 2048u * ks + 512u * d);
      SB();
      #pragma unroll
      for (int ks = 0; ks < 8; ++ks) {
        if (ks + 2 < 8) {
          #pragma unroll
          for (int d = 0; d < 2; ++d) fr[ks + 2][d] = lds_tr2(L, b0 + 2048u * (ks + 2) + 512u * d, b1 + 2048u * (ks + 2) + 512u * d);
        }
        if ((pi & 3) == 0 && ks == 0) qa[0] = MFMA(fr[ks][0], xf[8 * (pi & 3) + ks], f32x16{}); else qa[0] = MFMA(fr[ks][0], xf[8 * (pi & 3) + ks], qa[0]);
        SB();
        if ((pi & 3) == 0 && ks == 0) qa[1] = MFMA(fr[ks][1], xf[8 * (pi & 3) + ks], f32x16{}); else qa[1] = MFMA(fr[ks][1], xf[8 * (pi & 3) + ks], qa[1]);
        SB();
      }
      if ((pi & 3) == 3) {
        const u32 base = qh + 4096u * ((u32)(pi >> 2) ^ dh0);
        lds_w128(L, base, pack8(qa[0], 0, QSCALE));        lds_w128(L, base + 1024u, pack8(qa[0], 8, QSCALE));
        lds_w128(L, base + 2048u, pack8(qa[1], 0, QSCALE)); lds_w128(L, base + 3072u, pack8(qa[1], 8, QSCALE));
      }
      SB();
    };
    auto nodma = [&](int) {};
    constexpr u32 S0_ = 0, S2 = 2 * SLOT, S3 = 3 * SLOT, S4 = 4 * SLOT, S5 = 5 * SLOT, W0 = L_W, W1 = L_W + SLOT, QH0 = L_QH, QH1 = L_QH + SLOT;
    (void)S0_;
    issue_w(0, 0, W0); issue_w(0, 1, W1);
    barrier_vm<4>();
    STAMP(1);
    read_x(0, S2, S3); piece(0, W0, nodma);
    barrier_vm<0>();
    issue_w(0, 2, W0);
    read_x(1, S4, S5); piece(1, W1, nodma);
    barrier_vm<0>();
    STAMP(2);
    issue_w(0, 3, W1); issue_w(0, 4, S4); issue_w(0, 5, S5);
    read_x(2, QH0, QH1); piece(2, W0, nodma);
    barrier_vm<8>();
    issue_w(0, 6, W0);
    read_x(3, S2, S3); piece(3, W1, nodma);
    barrier_vm<8>();
    STAMP(3);
    issue_w(0, 7, W1);
    issue_kv(srdV, 0, L_V, vo_kv);
    piece(4, S4, nodma);
    barrier_vm<12>();
    issue_kv(srdV, 1, L_V + SLOT, vo_kv);
    piece(5, S5, nodma);
    barrier_vm<12>(); STAMP(4); piece(6, W0, nodma);
    barrier_vm<8>();
    issue_w(1, 0, W0);
    piece(7, W1, nodma);
    barrier_vm<0>();
    STAMP(5);
    bf16x8 og1[8], og2[8];
    #pragma unroll
    for (int i = 0; i < 8; ++i) og1[i] = bf16x8{};
    u32 s3 = 0;
    TICKDECL
    #pragma unroll 1
    for (int rd = 0; rd < 3; ++rd) {
      #pragma unroll
      for (int pi = 0; pi < 8; ++pi) {
        const u32 s = 8u * rd + pi, s1 = s + 1;
        const u32 vdst = L_V + ((s3 == 0) ? 2 : s3 - 1) * SLOT;
        auto dma = [&](const int jj) {
          if (jj < 4) w_one(1 + (s1 >> 3), s1 & 7, L_W + (s1 & 1) * SLOT, jj);
          else if (jj < 8) kv_one(srdV, s + 2, vdst, jj - 4, vo_kv);
        };
        if (pi == 1 && rd == 2) {
          #pragma unroll
          for (int i = 0; i < 8; ++i) og1[i] = lds_b128(L, qh + i * 1024u);
        }
        TICK(0);
        piece(pi, L_W + (pi & 1) * SLOT, dma);
        s3 = (s3 == 2) ? 0 : s3 + 1;
        TICK(1);
        barrier_vm<4>();
        TICK(3);
      }
    }
    #pragma unroll 1
    for (u32 s = 24; s < 32; ++s) {
      const u32 vdst = L_V + ((s3 == 0) ? 2 : s3 - 1) * SLOT;
      if (s + 2 < 32) issue_kv(srdV, s + 2, vdst, vo_kv);
      if (s == 25) {
        #pragma unroll
        for (int i = 0; i < 8; ++i) og2[i] = lds_b128(L, qh + i * 1024u);
      }
      s3 = (s3 == 2) ? 0 : s3 + 1;
      if (s < 30) barrier_vm<4>(); else barrier_vm<0>();
    }
    STAMP(6);
    TICKOUT
    #pragma unroll
    for (int ks = 0; ks < 8; ++ks) {
      lds_w128(L, 1 * 32768u + w * 8192u + ks * 1024u + ln * 16u, og1[ks]);
      lds_w128(L, 2 * 32768u + w * 8192u + ks * 1024u + ln * 16u, og2[ks]);
    }
  }

  {
    LANE_CONSTS
    const u32 nh = wave >> 2;
    const __amdgpu_buffer_rsrc_t srdO = mksrd(p.wo[br], NH * DH * DM * 2);
    const u32 vo_o = 1024u * ((ln >> 2) & 7) + cpart;
    auto o_one = [&](const int u, const int i) {
      const u32 hd = hg * 4 + ((((u & 7) >> 1) + hrot) & 3);
      const u32 so = (hd * 128u + 64u * (u & 1) + 8u * w) * 1024u + (256u * nh + 128u * (u >> 3)) * 2u;
      const u32 lb = ldsb + ((u + 4) % 5) * 32768u + nh * SLOT + 2048u * w;
      dma_w(rfl(lb + 1024u * (i & 1) + 8192u * (i >> 1)), vo_o, srdO, rfl(so + 128u * (i & 1) + 32768u * (i >> 1)));
    };
    #pragma unroll
    for (int i = 0; i < 4; ++i) o_one(0, i);
    barrier_lgkm();
    bf16x8 ob[32];
    #pragma unroll
    for (int i = 0; i < 32; ++i) ob[i] = lds_b128(L, (i >> 3) * 32768u + w * 8192u + (i & 7) * 1024u + ln * 16u);
    barrier_vm<0>();
    STAMP(7);
    #pragma unroll
    for (int i = 0; i < 4; ++i) o_one(1, i);
    #pragma unroll
    for (int i = 0; i < 4; ++i) o_one(2, i);
    const u32 vB0 = 64u * (4 * h + q4) + 16u * ((u32)(2 * blk + (p4 >> 1)) ^ (u32)h) + 8u * (p4 & 1);
    const u32 vB1 = 2048u + 64u * (4 * h + q4) + 16u * ((u32)(2 * blk + (p4 >> 1)) ^ (u32)(2 + h)) + 8u * (p4 & 1);
    char* part = (char*)p.ws + WS_P + ((size_t)((br * NB + b) * 8 + hg) * 8 + wave) * 16384 + ln * 16;
    const bool live = 32 * w + r < SQ;
    f32x16 acc[4];
    #pragma unroll
    for (int u = 0; u < 16; ++u) {
      u32 la = ((u + 4) % 5) * 32768u + nh * SLOT;
      OPAQUE_S(la);
      const u32 b0 = la + vB0, b1 = la + vB1;
      auto aread = [&](const int i) { return lds_tr2(L, b0 + 4096u * (i >> 2) + 512u * (i & 3), b1 + 4096u * (i >> 2) + 512u * (i & 3)); };
      bf16x8 af[16];
      #pragma unroll
      for (int i = 0; i < 6; ++i) af[i] = aread(i);
      SB();
      #pragma unroll
      for (int i = 0; i < 16; ++i) {
        const int ks = i >> 2, nt = i & 3;
        if (i + 6 < 16) af[i + 6] = aread(i + 6);
        const bf16x8 bfrag = ob[8 * ((u & 7) >> 1) + 4 * (u & 1) + ks];
        if ((u & 7) == 0 && ks == 0) acc[nt] = MFMA(af[i], bfrag, f32x16{}); else acc[nt] = MFMA(af[i], bfrag, acc[nt]);
        SB();
        if (u + 3 < 16 && (i & 3) == 1) o_one(u + 3, i >> 2);
      }
      if ((u & 7) == 7) {
        if (live) {
          #pragma unroll
          for (int nt = 0; nt < 4; ++nt) {
            __builtin_nontemporal_store(pack8n(acc[nt], 0), (u32x4*)(part + ((u >> 3) * 4 + nt) * 2048));
            __builtin_nontemporal_store(pack8n(acc[nt], 8), (u32x4*)(part + ((u >> 3) * 4 + nt) * 2048 + 1024));
          }
        }
      }
      if (u == 7 || u == 8 || u == 9) barrier_vm<16>();
      else if (u < 13) barrier_vm<8>();
      else if (u == 13) barrier_vm<4>();
      else if (u == 14) barrier_vm<0>();
    }
  }
  STAMP(10); STAMPC(9);
}

__global__ __launch_bounds__(256) void reduce_out(Params p) {
  const int tid = threadIdx.x, ln = tid & 63, sub = tid >> 6;
  const u32 blk = blockIdx.x;
  const u32 brb = blk >> 5, wave = (blk >> 2) & 7, T = (blk & 3) * 2 + (sub >> 1), sp = sub & 1;
  const u32 w = wave & 3, nh = wave >> 2, q = 32 * w + (ln & 31), h = ln >> 5;
  if (q >= SQ) return;
  const char* src = (const char*)p.ws + WS_P + ((size_t)brb * 64 + wave) * 16384 + T * 2048 + sp * 1024 + ln * 16;
  float a[8] = {};
  u32x4 v[8];
  #pragma unroll
  for (int g = 0; g < 8; ++g) v[g] = __builtin_nontemporal_load((const u32x4*)(src + (size_t)g * 8 * 16384));
  #pragma unroll
  for (int g = 0; g < 8; ++g)
    #pragma unroll
    for (int i = 0; i < 4; ++i) { a[2 * i] += __uint_as_float(v[g][i] << 16); a[2 * i + 1] += __uint_as_float(v[g][i] & 0xffff0000u); }
  const u32 n0 = 256 * nh + 128 * (T >> 2) + 32 * (T & 3) + 16 * sp;
  const u32 br = brb >> 4, b = brb & 15;
  __bf16* out = (__bf16*)p.out + ((size_t)b * SQ + q) * (2 * DM) + br * DM + n0 + 4 * h;
  typedef u32 u32x2 __attribute__((ext_vector_type(2)));
  *(u32x2*)(out) = u32x2{cvtpk(a[0], a[1]), cvtpk(a[2], a[3])};
  *(u32x2*)(out + 8) = u32x2{cvtpk(a[4], a[5]), cvtpk(a[6], a[7])};
}

extern "C" void kernel_launch(void* const* d_in, const int* in_sizes, int n_in, void* d_out, int out_size, void* d_ws, size_t ws_size,
                              hipStream_t stream) {
  Params p;
  for (int i = 0; i < 2; ++i) { p.x[i] = d_in[5 * i]; p.k[i] = d_in[5 * i + 1]; p.v[i] = d_in[5 * i + 2]; p.wq[i] = d_in[5 * i + 3]; p.wo[i] = d_in[5 * i + 4]; }
  p.out = d_out; p.ws = d_ws;
  (void)hipFuncSetAttribute((const void*)fused_attn, hipFuncAttributeMaxDynamicSharedMemorySize, LDS_BYTES);
  fused_attn<<<256, 512, LDS_BYTES, stream>>>(p);
  reduce_out<<<1024, 256, 0, stream>>>(p);
}
```

```cpp
#define XDEF
#include <hip/hip_runtime.h>
#include <hip/hip_bf16.h>

typedef unsigned u32;
typedef unsigned long long u64;
typedef __bf16 bf16x8 __attribute__((ext_vector_type(8)));
typedef __bf16 bf16x4 __attribute__((ext_vector_type(4)));
typedef __bf16 bf16x2 __attribute__((ext_vector_type(2)));
typedef float f32x4 __attribute__((ext_vector_type(4)));
typedef float f32x2 __attribute__((ext_vector_type(2)));
typedef u32 u32x4 __attribute__((ext_vector_type(4)));
typedef u32 u32x2 __attribute__((ext_vector_type(2)));
#define LAS __attribute__((address_space(3)))
#define DI __device__ __forceinline__

constexpr int NB = 16, SQ = 112, DM = 512, NH = 32, SK = 512, DH = 128;
constexpr float QSCALE = 0.12751743082f;
constexpr float THR = 8.0f;

constexpr u32 SLOT = 16384;
constexpr u32 L_K = 0;
constexpr u32 L_V = 3 * SLOT;
constexpr u32 L_W = 6 * SLOT;
constexpr u32 L_QH = 8 * SLOT;
constexpr u32 LDS_BYTES = 10 * SLOT;
constexpr u32 OREG = 28672;
constexpr u32 L_RING = 2 * OREG;
constexpr u32 PIECE = 4096;

constexpr size_t WS_P = 0;
constexpr u32 PART_WG = 8 * 7 * 2 * 1024;

struct Params {
  const void* x[2]; const void* k[2]; const void* v[2]; const void* wq[2]; const void* wo[2];
  void* out; void* ws;
};

DI u32 rfl(u32 x) { return __builtin_amdgcn_readfirstlane(x); }
DI __amdgpu_buffer_rsrc_t mksrd(const void* p, u32 bytes) { return __builtin_amdgcn_make_buffer_rsrc((void*)p, (short)0, (int)bytes, 0x00020000); }
DI void dma_nt(u32 m0v, u32 voff, __amdgpu_buffer_rsrc_t srd, u32 soff) {
  asm volatile("s_mov_b32 m0, %0\n\ts_nop 0\n\tbuffer_load_dwordx4 %1, %2, %3 offen nt lds" :: "s"(m0v), "v"(voff), "s"(srd), "s"(soff) : "memory");
}
#define WPOL0 ""
DI void dma_w(u32 m0v, u32 voff, __amdgpu_buffer_rsrc_t srd, u32 soff) {
  asm volatile("s_mov_b32 m0, %0\n\ts_nop 0\n\tbuffer_load_dwordx4 %1, %2, %3 offen" WPOL0 " lds" :: "s"(m0v), "v"(voff), "s"(srd), "s"(soff) : "memory");
}
#define KROTM 1
#define KROT (KROTM == 1 ? (((b >> 2) + 2 * (j & 1)) & 3) : 0)
#define DROT (KROTM == 1 ? ((b >> 1) & 1) : 0)

template <int N> DI void waitvm() { asm volatile("s_waitcnt vmcnt(%0)" :: "i"(N) : "memory"); }
template <int N> DI void waitlgkm() { __builtin_amdgcn_s_waitcnt(0xC07F | (N << 8)); }
DI void barrier_lgkm() { asm volatile("" ::: "memory"); waitlgkm<0>(); __builtin_amdgcn_s_barrier(); asm volatile("" ::: "memory"); }
template <int N> DI void barrier_vm() { asm volatile("s_waitcnt vmcnt(%0)" :: "i"(N) : "memory"); waitlgkm<0>(); __builtin_amdgcn_s_barrier(); asm volatile("" ::: "memory"); }
DI u32 cvtpk(float lo, float hi) { f32x2 v = {lo, hi}; bf16x2 b = __builtin_convertvector(v, bf16x2); return __builtin_bit_cast(u32, b); }
DI u32x2 pack4(const f32x4& a, float s) { return u32x2{cvtpk(a[0] * s, a[1] * s), cvtpk(a[2] * s, a[3] * s)}; }
DI u32x2 pack4n(const f32x4& a) { return u32x2{cvtpk(a[0], a[1]), cvtpk(a[2], a[3])}; }
template <class T> DI LAS T* lp(u32 off) { return (LAS T*)off; }
DI bf16x8 lds_b128(LAS char*, u32 off) { return *lp<bf16x8>(off); }
DI void lds_w128(LAS char*, u32 off, bf16x8 v) { *lp<bf16x8>(off) = v; }
DI void lds_w64(LAS char*, u32 off, u32x2 v) { *lp<u32x2>(off) = v; }
DI bf16x8 lds_tr2(LAS char*, u32 off0, u32 off1) {
  bf16x4 a = __builtin_amdgcn_ds_read_tr16_b64_v4bf16(lp<bf16x4>(off0));
  bf16x4 b = __builtin_amdgcn_ds_read_tr16_b64_v4bf16(lp<bf16x4>(off1));
  return __builtin_shufflevector(a, b, 0, 1, 2, 3, 4, 5, 6, 7);
}
#define STAMP(i)
#define STAMPC(i)
#define STAMPOUT()
#define SB() __builtin_amdgcn_sched_barrier(0)
#define TICK(i)
#define TICKDECL
#define TICKOUT
#define PIN(x) asm volatile("" : "+v"(x))
#define OPAQUE_S(x) asm volatile("" : "+s"(x))
DI float vmax3(float a, float b, float c) { return __builtin_elementwise_maximum(__builtin_elementwise_maximum(a, b), c); }
#define MFMA_ACC(acc, a, b) asm volatile("v_mfma_f32_16x16x32_bf16 %0, %1, %2, %0" : "+v"(acc) : "v"(a), "v"(b))
#define MFMA_INIT(acc, a, b, c) asm volatile("v_mfma_f32_16x16x32_bf16 %0, %1, %2, %3" : "=&v"(acc) : "v"(a), "v"(b), "v"(c))
#define MFMA_ZERO(acc, a, b) asm volatile("v_mfma_f32_16x16x32_bf16 %0, %1, %2, 0" : "=&v"(acc) : "v"(a), "v"(b))
#define NOP8() asm volatile("s_nop 7")
#define NOP2() asm volatile("s_nop 1")
#define EXP2(x) __builtin_amdgcn_exp2f(x)
DI int lane_id() { int ln; asm volatile("v_mbcnt_lo_u32_b32 %0, -1, 0\n\tv_mbcnt_hi_u32_b32 %0, -1, %0" : "=v"(ln)); return ln; }
DI float xg_max(float x) {
  auto a = __builtin_amdgcn_permlane16_swap(__float_as_uint(x), __float_as_uint(x), false, false);
  x = fmaxf(__uint_as_float(a[0]), __uint_as_float(a[1]));
  auto b = __builtin_amdgcn_permlane32_swap(__float_as_uint(x), __float_as_uint(x), false, false);
  return fmaxf(__uint_as_float(b[0]), __uint_as_float(b[1]));
}
DI float xg_sum(float x) {
  auto a = __builtin_amdgcn_permlane16_swap(__float_as_uint(x), __float_as_uint(x), false, false);
  x = __uint_as_float(a[0]) + __uint_as_float(a[1]);
  auto b = __builtin_amdgcn_permlane32_swap(__float_as_uint(x), __float_as_uint(x), false, false);
  return __uint_as_float(b[0]) + __uint_as_float(b[1]);
}

struct Ctx {
  LAS char* L; u32 ldsb, wave, w, br, b, hg, j;
  __amdgpu_buffer_rsrc_t srdK, srdV;
};

DI void issue_kv(Ctx& c, const __amdgpu_buffer_rsrc_t srd, u32 st, u32 la, u32 vo0, u32 vo1, u32 vo2, u32 vo3) {
  asm volatile("s_mov_b32 m0, %0\n\ts_nop 0\n\t"
               "buffer_load_dwordx4 %1, %5, %6 offen nt lds\n\t"
               "buffer_load_dwordx4 %2, %5, %6 offen offset:1024 nt lds\n\t"
               "buffer_load_dwordx4 %3, %5, %6 offen offset:2048 nt lds\n\t"
               "buffer_load_dwordx4 %4, %5, %6 offen offset:3072 nt lds"
               :: "s"(rfl(c.ldsb + la + 4096u * c.w)), "v"(vo0), "v"(vo1), "v"(vo2), "v"(vo3), "s"(srd), "s"(rfl(st * SLOT + 4096u * c.w)) : "memory");
}

DI u32 wq_pattern(const Ctx& c, int ln) { return 8192u * (ln >> 3) + 16u * ((ln & 7) ^ (2u * (((ln >> 4) & 1) + 2u * (c.w & 1)))); }
DI void issue_wq(const Ctx& c, const __amdgpu_buffer_rsrc_t srdW, u32 vo_w, u32 rdn, u32 pi, u32 la) {
  const u32 b = c.b, j = c.j; (void)j; (void)b;
  const u32 hd = c.hg * 4 + rdn;
  const u32 kq = ((pi & 3) + KROT) & 3, dh = (pi >> 2) ^ DROT;
  const u32 so = (128u * kq + 8u * c.w) * 8192u + (128u * hd + 64u * dh) * 2u;
  #pragma unroll
  for (int i = 0; i < 4; ++i) dma_w(rfl(c.ldsb + la + 1024u * c.w + 4096u * i), vo_w, srdW, rfl(so + 32u * 8192u * i));
}
DI void issue_x_rows(const Params& p, Ctx& c, int ln) {
  const __amdgpu_buffer_rsrc_t srdX = mksrd((const char*)p.x[c.br] + (size_t)c.b * SQ * DM * 2, SQ * DM * 2);
  const u32 start = 56u * (c.j & 1);
  #pragma unroll
  for (int i = 0; i < 14; ++i) {
    u32 row = start + c.wave + 8u * i; row = row >= 112u ? row - 112u : row;
    dma_w(rfl(c.ldsb + 1024u * row), (16u * ln) ^ rfl(16u * (row & 15u)), srdX, rfl(1024u * row));
  }
}

template <int NQ>
DI void attention_role(const Params& p, Ctx& c, bf16x8 (&park)[16]) {
  LAS char* L = c.L;
  const u32 w = c.w, wave = c.wave; (void)wave;
  const int ln = lane_id();
  const u32 cl = ln & 15, g = ln >> 4, q4 = cl >> 2, p4 = ln & 3;
  const u32 pi_ = (cl & 3) | ((cl & 4) << 1) | ((cl & 8) >> 1);
  const u32 vk0 = 256u * g + 16u * (cl ^ g), vk1 = vk0 ^ 64u, vk2 = vk0 ^ 128u, vk3 = vk0 ^ 192u;
  const u32 kA = 256u * pi_ + 16u * (g ^ pi_);
  const u32 vA = 256u * (8u * (g & 1) + 4u * (g >> 1) + q4) + 16u * ((2u * (q4 + 4u * (g & 1))) | (p4 >> 1)) + 8u * (p4 & 1);
  const u32 rowq = L_QH + 256u * (32u * w + cl);
  const u32 qA = rowq + 16u * (g ^ cl);
  const u32 oW = 16u * ((g >> 1) ^ cl) + 8u * (g & 1);
  {
    const __amdgpu_buffer_rsrc_t srdW = mksrd(p.wq[c.br], DM * NH * DH * 2);
    const u32 vo_w = wq_pattern(c, ln);
    issue_x_rows(p, c, ln);
    barrier_vm<0>();
    barrier_lgkm();
    issue_wq(c, srdW, vo_w, 0, 3, 3 * SLOT); issue_wq(c, srdW, vo_w, 0, 4, 4 * SLOT);
    barrier_lgkm(); barrier_lgkm();
    waitvm<4>(); issue_wq(c, srdW, vo_w, 0, 5, 5 * SLOT);
    barrier_lgkm();
    waitvm<4>(); issue_wq(c, srdW, vo_w, 0, 6, 6 * SLOT);
    barrier_lgkm();
    waitvm<4>(); issue_kv(c, c.srdK, 0, L_K, vk0, vk1, vk2, vk3);
    barrier_lgkm();
    waitvm<4>(); issue_kv(c, c.srdK, 1, L_K + SLOT, vk0, vk1, vk2, vk3);
    barrier_lgkm();
    waitvm<4>();
    barrier_lgkm();
  }
  bf16x8 kf[16];
  #pragma unroll
  for (int i = 0; i < 16; ++i) kf[i] = lds_b128(L, L_K + (kA ^ (64u * (i & 3))) + 4096u * (i >> 2));
  barrier_vm<0>();
  issue_kv(c, c.srdK, 2, L_K + 2 * SLOT, vk0, vk1, vk2, vk3);
  STAMP(1);
  bf16x8 qf[NQ][4];
  #pragma unroll
  for (int i = 0; i < NQ; ++i)
    #pragma unroll
    for (int ks = 0; ks < 4; ++ks) qf[i][ks] = lds_b128(L, (qA ^ (64u * ks)) + 4096u * i);
  waitlgkm<0>();
  if (NQ == 1) {
    #pragma unroll
    for (int i = 0; i < 16; ++i) park[i] = bf16x8{};
  }
  bf16x8 ones = __builtin_bit_cast(bf16x8, u32x4{0x3F803F80u, 0x3F803F80u, 0x3F803F80u, 0x3F803F80u});
  PIN(ones);
  u32 s3 = 0;
  TICKDECL
  #pragma unroll 1
  for (int rd = 0; rd < 4; ++rd) {
    f32x4 O[8][NQ];
    #pragma unroll
    for (int d = 0; d < 8; ++d)
      #pragma unroll
      for (int i = 0; i < NQ; ++i) O[d][i] = f32x4{};
    float m[NQ];
    f32x4 negm[NQ], l[NQ];
    #pragma unroll
    for (int i = 0; i < NQ; ++i) { m[i] = 0.f; l[i] = f32x4{}; negm[i] = f32x4{}; }
    #pragma unroll 1
    for (int t = 0; t < 8; ++t) {
      const u32 s3n = (s3 == 2) ? 0 : s3 + 1;
      const u32 vbase = L_V + s3 * SLOT, kbase = L_K + s3n * SLOT;
      const u32 sK = 8u * rd + t + 3;
      const bool kdma = sK < 32;
      TICK(0);
      if (kdma) issue_kv(c, c.srdK, sK, L_K + s3 * SLOT, vk0, vk1, vk2, vk3);
      TICK(1);
      if (NQ == 1 && rd == 1 && t == 1) {
        #pragma unroll
        for (int i = 0; i < 16; ++i) park[i] = lds_b128(L, ((L_QH + 256u * cl + 16u * (g ^ cl)) ^ (64u * (i & 3))) + 4096u * ((i >> 2) == 3 ? 6 : (i >> 2)));
      }
      u32 va[8];
      va[0] = vbase + vA;
      #pragma unroll
      for (int d = 1; d < 8; ++d) va[d] = va[0] ^ (32u * d);
      auto vread = [&](const int d, const int u) { return lds_tr2(L, va[d] + 8192u * u, va[d] + 8192u * u + 4096u); };
      f32x4 S[4][NQ];
      bf16x8 vf[16];
      #pragma unroll
      for (int kt = 0; kt < 2; ++kt)
        #pragma unroll
        for (int ks = 0; ks < 4; ++ks) {
          #pragma unroll
          for (int i = 0; i < NQ; ++i) { if (ks == 0) MFMA_INIT(S[kt][i], kf[4 * kt + ks], qf[i][ks], negm[i]); else MFMA_ACC(S[kt][i], kf[4 * kt + ks], qf[i][ks]); }
          vf[4 * kt + ks] = vread(4 * kt + ks, 0);
          SB();
        }
      u32x4 pw[NQ][2];
      float pa[NQ];
      #pragma unroll
      for (int kt = 2; kt < 4; ++kt)
        #pragma unroll
        for (int ks = 0; ks < 4; ++ks) {
          const int n = 4 * (kt - 2) + ks;
          #pragma unroll
          for (int i = 0; i < NQ; ++i) { if (ks == 0) MFMA_INIT(S[kt][i], kf[4 * kt + ks], qf[i][ks], negm[i]); else MFMA_ACC(S[kt][i], kf[4 * kt + ks], qf[i][ks]); }
          vf[8 + n] = vread(n, 1);
          if (n >= 2 && n < 6) {
            const int k2 = (n - 2) >> 1, e = 2 * ((n - 2) & 1);
            #pragma unroll
            for (int i = 0; i < NQ; ++i) {
              const float x0 = S[k2][i][e], x1 = S[k2][i][e + 1];
              pa[i] = (n == 2) ? __builtin_elementwise_maximum(x0, x1) : vmax3(pa[i], x0, x1);
              u32 pk = cvtpk(EXP2(x0), EXP2(x1)); PIN(pk);
              pw[i][0][2 * k2 + (e >> 1)] = pk;
            }
          }
          SB();
        }
      asm volatile("" ::: "memory");
      NOP8();
      SB();
      TICK(2);
      bool grow = false;
      #pragma unroll
      for (int i = 0; i < NQ; ++i) {
        const float pb = vmax3(S[2][i][0], S[2][i][1], S[2][i][2]);
        pa[i] = vmax3(pa[i], S[2][i][3], S[3][i][0]); pa[i] = vmax3(pa[i], S[3][i][1], S[3][i][2]);
        pa[i] = vmax3(pa[i], pb, S[3][i][3]);
      }
      grow = (NQ == 2 ? __builtin_elementwise_maximum(pa[0], pa[NQ - 1]) : pa[0]) > THR;
      if (__builtin_expect(t == 0 || __any(grow), 0)) {
        #pragma unroll
        for (int i = 0; i < NQ; ++i) {
          const float pmx = xg_max(pa[i]);
          const float delta = (t == 0) ? pmx : fmaxf(pmx, 0.f);
          const float alpha = (t == 0) ? 0.f : EXP2(-delta);
          m[i] += delta; l[i] *= alpha;
          negm[i] = f32x4{-m[i], -m[i], -m[i], -m[i]};
          #pragma unroll
          for (int kt = 0; kt < 4; ++kt) S[kt][i] -= delta;
          #pragma unroll
          for (int d = 0; d < 8; ++d) O[d][i] *= alpha;
          #pragma unroll
          for (int k2 = 0; k2 < 2; ++k2)
            #pragma unroll
            for (int e = 0; e < 4; e += 2) pw[i][0][2 * k2 + (e >> 1)] = cvtpk(EXP2(S[k2][i][e]), EXP2(S[k2][i][e + 1]));
        }
        SB();
        NOP2();
      }
      waitlgkm<15>();
      SB();
      TICK(3);
      #pragma unroll
      for (int i = 0; i < NQ; ++i) MFMA_ACC(l[i], ones, __builtin_bit_cast(bf16x8, pw[i][0]));
      #pragma unroll
      for (int d = 0; d < 8; ++d) {
        #pragma unroll
        for (int i = 0; i < NQ; ++i) MFMA_ACC(O[d][i], vf[d], __builtin_bit_cast(bf16x8, pw[i][0]));
        if (d < 4) {
          const int k2 = 2 + (d >> 1), e = 2 * (d & 1);
          #pragma unroll
          for (int i = 0; i < NQ; ++i) { u32 pk = cvtpk(EXP2(S[k2][i][e]), EXP2(S[k2][i][e + 1])); PIN(pk); pw[i][1][2 * (k2 - 2) + (e >> 1)] = pk; }
        }
        SB();
      }
      u32 ka[4];
      ka[0] = kbase + kA;
      #pragma unroll
      for (int ks = 1; ks < 4; ++ks) ka[ks] = ka[0] ^ (64u * ks);
      waitlgkm<0>();
      SB();
      #pragma unroll
      for (int i = 0; i < NQ; ++i) MFMA_ACC(l[i], ones, __builtin_bit_cast(bf16x8, pw[i][1]));
      #pragma unroll
      for (int d = 0; d < 8; ++d) {
        #pragma unroll
        for (int i = 0; i < NQ; ++i) MFMA_ACC(O[d][i], vf[8 + d], __builtin_bit_cast(bf16x8, pw[i][1]));
        SB();
        kf[2 * d] = lds_b128(L, ka[(2 * d) & 3] + 4096u * ((2 * d) >> 2));
        kf[2 * d + 1] = lds_b128(L, ka[(2 * d + 1) & 3] + 4096u * ((2 * d + 1) >> 2));
        SB();
      }
      s3 = s3n;
      TICK(4);
      if (kdma) barrier_vm<4>(); else barrier_vm<0>();
      TICK(5);
    }
    if (rd < 3) {
      #pragma unroll
      for (int i = 0; i < NQ; ++i)
        #pragma unroll
        for (int ks = 0; ks < 4; ++ks) qf[i][ks] = lds_b128(L, (qA ^ (64u * ks)) + 4096u * i);
    }
    const u32 orow = rowq - ((rd == 3) ? L_QH : 0u);
    NOP8();
    SB();
    #pragma unroll
    for (int i = 0; i < NQ; ++i) {
      const float inv = 1.0f / l[i][0];
      #pragma unroll
      for (int d = 0; d < 8; ++d) lds_w64(L, orow + 4096u * i + (oW ^ (32u * d)), pack4(O[d][i], inv));
    }
    waitlgkm<0>();
    STAMP(2 + rd);
  }
  TICKOUT
}

template <int NQ>
DI void projection_role(const Params& p, Ctx& c, bf16x8 (&park)[16]) {
  LAS char* L = c.L;
  const u32 w = c.w, wave = c.wave; (void)wave;
  const u32 b = c.b, j = c.j; (void)j;
  const int ln = lane_id();
  const u32 cl = ln & 15, g = ln >> 4, q4 = cl >> 2, p4 = ln & 3;
  const u32 vv0 = 256u * g + 16u * (cl ^ (2u * g)), vv2 = vv0 ^ 128u;
  const __amdgpu_buffer_rsrc_t srdW = mksrd(p.wq[c.br], DM * NH * DH * 2);
  const u32 rot = KROT, dh0 = DROT;
  const u32 vo_w = wq_pattern(c, ln);
  auto issue_w = [&](u32 rdn, u32 pi, u32 la) { issue_wq(c, srdW, vo_w, rdn, pi, la); };
  bf16x8 xf[NQ][16];
  const u32 wA = 128u * (8u * g + q4) + 16u * ((2u * ((q4 >> 1) + 2u * (g & 1))) | (p4 >> 1)) + 8u * (p4 & 1);
  const u32 rowq = L_QH + 256u * (32u * w + cl);
  const u32 qA = rowq + 16u * (g ^ cl);
  const u32 oW = 16u * ((g >> 1) ^ cl) + 8u * (g & 1);
  f32x4 qa[4][NQ];
  auto piece = [&](const int kq, u32 la, u32 dh) {
    OPAQUE_S(la);
    u32 wa[4];
    #pragma unroll
    for (int d = 0; d < 4; ++d) wa[d] = la + (wA ^ (32u * d));
    auto wread = [&](const int n) { return lds_tr2(L, wa[n & 3] + 4096u * (n >> 2), wa[n & 3] + 4096u * (n >> 2) + 512u); };
    bf16x8 fr[16];
    #pragma unroll
    for (int n = 0; n < 6; ++n) fr[n] = wread(n);
    SB();
    #pragma unroll
    for (int hp = 0; hp < 8; ++hp) {
      if (hp < 6) waitlgkm<8>(); else if (hp == 6) waitlgkm<4>(); else waitlgkm<0>();
      SB();
      #pragma unroll
      for (int e = 0; e < 2; ++e) {
        const int n = 2 * hp + e, ks = n >> 2, d = n & 3;
        #pragma unroll
        for (int i = 0; i < NQ; ++i) { if (kq == 0 && ks == 0) MFMA_ZERO(qa[d][i], fr[n], xf[i][4 * kq + ks]); else MFMA_ACC(qa[d][i], fr[n], xf[i][4 * kq + ks]); }
      }
      SB();
      if (hp + 3 < 8) { fr[2 * hp + 6] = wread(2 * hp + 6); fr[2 * hp + 7] = wread(2 * hp + 7); }
      SB();
    }
    if (kq == 3) {
      NOP8();
      SB();
      #pragma unroll
      for (int i = 0; i < NQ; ++i)
        #pragma unroll
        for (int d = 0; d < 4; ++d) lds_w64(L, rowq + 4096u * i + (oW ^ (32u * (4u * dh + d))), pack4(qa[d][i], QSCALE));
    }
  };
  constexpr u32 S2 = 2 * SLOT, S3 = 3 * SLOT, S4 = 4 * SLOT, S5 = 5 * SLOT, S6 = 6 * SLOT, S7 = 7 * SLOT, S8 = 8 * SLOT, S9 = 9 * SLOT;
  STAMP(13);
  issue_w(0, 0, S7); issue_x_rows(p, c, ln); issue_w(0, 1, S8); issue_w(0, 2, S9);
  STAMP(14);
  barrier_vm<8>();
  STAMP(1);
  {
    const u32 xA = 1024u * (32u * w + cl) + 16u * (g ^ cl);
    #pragma unroll
    for (int jq = 0; jq < 4; ++jq) {
      const u32 a = xA + 256u * (((u32)jq + rot) & 3u);
      #pragma unroll
      for (int i = 0; i < NQ; ++i)
        #pragma unroll
        for (int ks = 0; ks < 4; ++ks) xf[i][4 * jq + ks] = lds_b128(L, (a ^ (64u * ks)) + 16384u * i);
    }
  }
  barrier_lgkm();
  piece(0, S7, dh0);
  barrier_vm<4>();
  STAMP(2);
  issue_w(1, 0, S7);
  piece(1, S8, dh0);
  barrier_vm<4>();
  issue_w(0, 7, S2);
  piece(2, S9, dh0);
  barrier_lgkm(); STAMP(3); piece(3, S3, dh0);
  barrier_lgkm();
  issue_kv(c, c.srdV, 0, L_V, vv0, vv0, vv2, vv2);
  piece(0, S4, dh0 ^ 1);
  barrier_lgkm();
  issue_kv(c, c.srdV, 1, L_V + SLOT, vv0, vv0, vv2, vv2);
  piece(1, S5, dh0 ^ 1);
  barrier_lgkm(); STAMP(4); piece(2, S6, dh0 ^ 1);
  barrier_vm<8>();
  piece(3, S2, dh0 ^ 1);
  barrier_vm<4>();
  STAMP(5);
  bf16x8 og1[NQ][4];
  #pragma unroll
  for (int i = 0; i < NQ; ++i)
    #pragma unroll
    for (int ks = 0; ks < 4; ++ks) og1[i][ks] = bf16x8{};
  if (NQ == 1) {
    #pragma unroll
    for (int i = 4; i < 16; ++i) park[i] = bf16x8{};
  }
  u32 s3 = 0;
  TICKDECL
  #pragma unroll 1
  for (int hh = 0; hh < 6; ++hh) {
    #pragma unroll
    for (int kq = 0; kq < 4; ++kq) {
      const u32 s = 4u * hh + kq, s1 = s + 1;
      const u32 vdst = L_V + ((s3 == 0) ? 2 : s3 - 1) * SLOT;
      TICK(0);
      issue_w((1 + (s1 >> 3)) & 3, s1 & 7, L_W + (kq & 1) * SLOT);
      TICK(1);
      if (NQ == 1 && kq == 1 && hh == 2) {
        #pragma unroll
        for (int i = 4; i < 16; ++i) park[i] = lds_b128(L, ((L_QH + 256u * cl + 16u * (g ^ cl)) ^ (64u * (i & 3))) + 4096u * (2 + (i >> 2)));
      }
      if (kq == 1 && hh == 4) {
        #pragma unroll
        for (int i = 0; i < NQ; ++i)
          #pragma unroll
          for (int ks = 0; ks < 4; ++ks) og1[i][ks] = lds_b128(L, (qA ^ (64u * ks)) + 4096u * i);
      }
      piece(kq, L_W + ((kq + 1) & 1) * SLOT, (u32)(hh & 1) ^ dh0);
      issue_kv(c, c.srdV, s + 2, vdst, vv0, vv0, vv2, vv2);
      s3 = (s3 == 2) ? 0 : s3 + 1;
      TICK(2);
      barrier_vm<4>();
      TICK(4);
    }
  }
  #pragma unroll
  for (int i = 0; i < 4 * NQ; ++i) park[i] = bf16x8{};
  const __amdgpu_buffer_rsrc_t srdO = mksrd(p.wo[c.br], NH * DH * DM * 2);
  #pragma unroll 1
  for (u32 s = 24; s < 32; ++s) {
    const u32 vdst = L_V + ((s3 == 0) ? 2 : s3 - 1) * SLOT;
    if (s + 2 < 32) issue_kv(c, c.srdV, s + 2, vdst, vv0, vv0, vv2, vv2);
    dma_w(rfl(c.ldsb + L_W + 1024u * w), 16u * ln, srdO, rfl((c.hg * 512u + 32u * b + 4u * (s - 24) + w) * 1024u));
    if (s == 25) {
      #pragma unroll
      for (int i = 0; i < NQ; ++i)
        #pragma unroll
        for (int ks = 0; ks < 4; ++ks) park[4 * i + ks] = lds_b128(L, (qA ^ (64u * ks)) + 4096u * i);
    }
    s3 = (s3 == 2) ? 0 : s3 + 1;
    if (s < 30) barrier_vm<5>(); else barrier_vm<0>();
  }
  STAMP(6);
  TICKOUT
  #pragma unroll
  for (int i = 0; i < NQ; ++i)
    #pragma unroll
    for (int ks = 0; ks < 4; ++ks) lds_w128(L, OREG + ((qA - L_QH) ^ (64u * ks)) + 4096u * i, og1[i][ks]);
}

__global__ __launch_bounds__(512, 2) void fused_attn(Params p) {
  extern __shared__ __attribute__((aligned(16))) char smem[];
  Ctx c;
  c.L = (LAS char*)smem;
  c.ldsb = (u32)(size_t)smem;
  c.wave = rfl(threadIdx.x >> 6);
  c.w = c.wave & 3;
  const u32 wave = c.wave, w = c.w;
  const u32 bid = blockIdx.x, xcd = bid & 7;
  c.j = bid >> 3;
  c.br = xcd & 1; c.hg = 2 * (xcd >> 1) + (c.j & 1); c.b = c.j >> 1;
  const size_t kvoff = (size_t)((c.b * NH + c.hg * 4) * SK) * DH * 2;
  c.srdK = mksrd((const char*)p.k[c.br] + kvoff, 4 * SK * DH * 2);
  c.srdV = mksrd((const char*)p.v[c.br] + kvoff, 4 * SK * DH * 2);
  STAMP(0); STAMPC(8);
  bf16x8 park[16];
  if (wave < 3) attention_role<2>(p, c, park);
  else if (wave == 3) attention_role<1>(p, c, park);
  else if (wave < 7) projection_role<2>(p, c, park);
  else projection_role<1>(p, c, park);

  {
    LAS char* L = c.L;
    const int ln = lane_id();
    const u32 cl = ln & 15, g = ln >> 4, q4 = cl >> 2, p4 = ln & 3;
    const __amdgpu_buffer_rsrc_t srdO = mksrd(p.wo[c.br], NH * DH * DM * 2);
    const u32 vo0 = 1024u * (ln >> 3) + 16u * ((ln & 7) ^ (2u * ((ln >> 4) & 1))), vo1 = 1024u * (ln >> 3) + 16u * ((ln & 7) ^ (2u * (((ln >> 4) & 1) + 2u)));
    const u32 ring = L_RING + wave * (3 * PIECE);
    auto issue_o = [&](u32 u, u32 slot3) {
      const u32 hs = (0x2013u >> (4u * ((u >> 2) & 3))) & 3u;
      const u32 so = (u < 16) ? ((c.hg * 4 + hs) * 128u + 32u * (u & 3)) * 1024u + 128u * wave : 0x01000000u;
      const u32 lb = c.ldsb + ring + slot3 * PIECE;
      dma_w(rfl(lb), vo0, srdO, rfl(so));                     dma_w(rfl(lb + 1024u), vo1, srdO, rfl(so + 8192u));
      dma_w(rfl(lb + 2048u), vo0, srdO, rfl(so + 16384u));     dma_w(rfl(lb + 3072u), vo1, srdO, rfl(so + 24576u));
    };
    issue_o(0, 0); issue_o(1, 1); issue_o(2, 2);
    barrier_lgkm();
    STAMP(7);
    const u32 oA = 256u * cl + 16u * (g ^ cl);
    const u32 wA = 128u * (8u * g + q4) + 16u * ((2u * ((q4 >> 1) + 2u * (g & 1))) | (p4 >> 1)) + 8u * (p4 & 1);
    auto aread = [&](bf16x8 (&af)[4], u32 slot3) {
      const u32 a0 = ring + slot3 * PIECE + wA;
      #pragma unroll
      for (int nt = 0; nt < 4; ++nt) { const u32 a = a0 ^ (32u * nt); af[nt] = lds_tr2(L, a, a + 512u); }
    };
    f32x4 acc[4][7];
    #pragma unroll
    for (int nt = 0; nt < 4; ++nt)
      #pragma unroll
      for (int qt = 0; qt < 7; ++qt) acc[nt][qt] = f32x4{};
    bf16x8 afA[4], afB[4];
    SB();
    waitvm<8>();
    aread(afA, 0);
    u32 sl = 0;
    auto kstep = [&](u32 u, bf16x8 (&af)[4], bf16x8 (&afn)[4]) {
      const u32 sl1 = (sl == 2) ? 0 : sl + 1;
      const u32 ob = OREG * ((u >> 2) & 1) + (oA ^ (64u * (u & 3)));
      bf16x8 bq[7];
      #pragma unroll
      for (int qt = 0; qt < 3; ++qt) bq[qt] = lds_b128(L, ob + 4096u * qt);
      SB();
      #pragma unroll
      for (int qt = 0; qt < 7; ++qt) {
        if (qt == 1) issue_o(u + 3, sl);
        if (qt + 3 < 7) bq[qt + 3] = lds_b128(L, ob + 4096u * (qt + 3));
        if (qt == 3) { waitvm<8>(); aread(afn, sl1); }
        #pragma unroll
        for (int nt = 0; nt < 4; ++nt) MFMA_ACC(acc[nt][qt], af[nt], bq[qt]);
        SB();
      }
      sl = sl1;
    };
    #pragma unroll 1
    for (u32 u = 0; u < 16; u += 2) {
      if (u == 8) {
        barrier_lgkm();
        if (wave == 3 || wave == 7) {
          #pragma unroll
          for (int i = 0; i < 16; ++i) {
            if (wave == 3) lds_w128(L, (oA ^ (64u * (i & 3))) + 4096u * ((i >> 2) == 3 ? 6 : (i >> 2)), park[i]);
            else if (i >= 4) lds_w128(L, (oA ^ (64u * (i & 3))) + 4096u * (2 + (i >> 2)), park[i]);
          }
        }
        if (wave >= 4) {
          #pragma unroll
          for (int ks = 0; ks < 4; ++ks) {
            lds_w128(L, OREG + (oA ^ (64u * ks)) + 8192u * w, park[ks]);
            if (w < 3) lds_w128(L, OREG + (oA ^ (64u * ks)) + 8192u * w + 4096u, park[4 + ks]);
          }
        }
        barrier_lgkm();
      }
      if (u == 8) STAMP(12);
      kstep(u, afA, afB);
      kstep(u + 1, afB, afA);
    }
    waitvm<0>();
    STAMP(11);
    NOP8();
    SB();
    char* part = (char*)p.ws + WS_P + (size_t)((c.br * NB + c.b) * 8 + c.hg) * PART_WG + wave * 14336u + ln * 16;
    #pragma unroll
    for (int qt = 0; qt < 7; ++qt)
      #pragma unroll
      for (int np = 0; np < 2; ++np) {
        const u32x2 a = pack4n(acc[2 * np][qt]), bb = pack4n(acc[2 * np + 1][qt]);
        __builtin_nontemporal_store(u32x4{a[0], a[1], bb[0], bb[1]}, (u32x4*)(part + (qt * 2 + np) * 1024));
      }
  }
  STAMP(10); STAMPC(9);
  STAMPOUT();
}

__global__ __launch_bounds__(128) void reduce_out(Params p) {
  __shared__ __attribute__((aligned(16))) char tile[16 * 128];
  const int tid = threadIdx.x, ln = tid & 63, np = tid >> 6;
  const u32 blk = blockIdx.x;
  const u32 brb = blk / 56, rem = blk % 56, v = rem / 7, qt = rem % 7;
  const char* src = (const char*)p.ws + WS_P + (size_t)brb * 8 * PART_WG + v * 14336u + (qt * 2 + np) * 1024 + ln * 16;
  u32x4 x[8];
  #pragma unroll
  for (int gq = 0; gq < 8; ++gq) x[gq] = __builtin_nontemporal_load((const u32x4*)(src + (size_t)gq * PART_WG));
  float a[8] = {};
  #pragma unroll
  for (int gq = 0; gq < 8; ++gq)
    #pragma unroll
    for (int i = 0; i < 4; ++i) { a[2 * i] += __uint_as_float(x[gq][i] << 16); a[2 * i + 1] += __uint_as_float(x[gq][i] & 0xffff0000u); }
  const u32 cl = ln & 15, g = ln >> 4;
  *(u32x2*)(tile + cl * 128 + 64 * np + 8 * g) = u32x2{cvtpk(a[0], a[1]), cvtpk(a[2], a[3])};
  *(u32x2*)(tile + cl * 128 + 64 * np + 32 + 8 * g) = u32x2{cvtpk(a[4], a[5]), cvtpk(a[6], a[7])};
  __syncthreads();
  const u32 row = tid >> 3, ch = tid & 7, br = brb >> 4, b = brb & 15;
  char* out = (char*)p.out + (((size_t)b * SQ + 16 * qt + row) * (2 * DM) + br * DM + 64 * v) * 2 + 16 * ch;
  *(u32x4*)out = *(const u32x4*)(tile + row * 128 + 16 * ch);
}

extern "C" void kernel_launch(void* const* d_in, const int* in_sizes, int n_in, void* d_out, int out_size, void* d_ws, size_t ws_size,
                              hipStream_t stream) {
  Params p;
  for (int i = 0; i < 2; ++i) { p.x[i] = d_in[5 * i]; p.k[i] = d_in[5 * i + 1]; p.v[i] = d_in[5 * i + 2]; p.wq[i] = d_in[5 * i + 3]; p.wo[i] = d_in[5 * i + 4]; }
  p.out = d_out; p.ws = d_ws;
  (void)hipFuncSetAttribute((const void*)fused_attn, hipFuncAttributeMaxDynamicSharedMemorySize, LDS_BYTES);
  fused_attn<<<256, 512, LDS_BYTES, stream>>>(p);
  reduce_out<<<32 * 56, 128, 0, stream>>>(p);
}
```

```cpp
#define PFP 8
#include <hip/hip_runtime.h>
#include <hip/hip_bf16.h>

typedef unsigned u32;
typedef unsigned long long u64;
typedef __bf16 bf16x8 __attribute__((ext_vector_type(8)));
typedef __bf16 bf16x4 __attribute__((ext_vector_type(4)));
typedef __bf16 bf16x2 __attribute__((ext_vector_type(2)));
typedef float f32x4 __attribute__((ext_vector_type(4)));
typedef float f32x2 __attribute__((ext_vector_type(2)));
typedef u32 u32x4 __attribute__((ext_vector_type(4)));
typedef u32 u32x2 __attribute__((ext_vector_type(2)));
#define LAS __attribute__((address_space(3)))
#define DI __device__ __forceinline__

constexpr int NB = 16, SQ = 112, DM = 512, NH = 32, SK = 512, DH = 128;
constexpr float QSCALE = 0.12751743082f;
constexpr float THR = 8.0f;

constexpr u32 SLOT = 16384;
constexpr u32 L_K = 0;
constexpr u32 L_V = 3 * SLOT;
constexpr u32 L_W = 6 * SLOT;
constexpr u32 L_QH = 8 * SLOT;
constexpr u32 LDS_BYTES = 10 * SLOT;
constexpr u32 OREG = 28672;
constexpr u32 L_RING = 2 * OREG;
constexpr u32 PIECE = 4096;

constexpr size_t WS_P = 0;
constexpr u32 PART_WG = 8 * 7 * 2 * 1024;

struct Params {
  const void* x[2]; const void* k[2]; const void* v[2]; const void* wq[2]; const void* wo[2];
  void* out; void* ws;
};

DI u32 rfl(u32 x) { return __builtin_amdgcn_readfirstlane(x); }
DI __amdgpu_buffer_rsrc_t mksrd(const void* p, u32 bytes) { return __builtin_amdgcn_make_buffer_rsrc((void*)p, (short)0, (int)bytes, 0x00020000); }
DI void dma_nt(u32 m0v, u32 voff, __amdgpu_buffer_rsrc_t srd, u32 soff) {
  asm volatile("s_mov_b32 m0, %0\n\ts_nop 0\n\tbuffer_load_dwordx4 %1, %2, %3 offen nt lds" :: "s"(m0v), "v"(voff), "s"(srd), "s"(soff) : "memory");
}
#define WPOL0 ""
#define WPOL1 ""
DI void dma_wo(u32 m0v, u32 voff, __amdgpu_buffer_rsrc_t srd, u32 soff) {
  asm volatile("s_mov_b32 m0, %0\n\ts_nop 0\n\tbuffer_load_dwordx4 %1, %2, %3 offen" WPOL1 " lds" :: "s"(m0v), "v"(voff), "s"(srd), "s"(soff) : "memory");
}
DI void dma_w(u32 m0v, u32 voff, __amdgpu_buffer_rsrc_t srd, u32 soff) {
  asm volatile("s_mov_b32 m0, %0\n\ts_nop 0\n\tbuffer_load_dwordx4 %1, %2, %3 offen" WPOL0 " lds" :: "s"(m0v), "v"(voff), "s"(srd), "s"(soff) : "memory");
}
#define KROTM 1
#define PF 3
#define KPOS 1
#define KROT (KROTM == 1 ? (((b >> 2) + 2 * (j & 1)) & 3) : 0)
#define DROT (KROTM == 1 ? ((b >> 1) & 1) : 0)

template <int N> DI void waitvm() { asm volatile("s_waitcnt vmcnt(%0)" :: "i"(N) : "memory"); }
template <int N> DI void waitlgkm() { __builtin_amdgcn_s_waitcnt(0xC07F | (N << 8)); }
DI void barrier_lgkm() { asm volatile("" ::: "memory"); waitlgkm<0>(); __builtin_amdgcn_s_barrier(); asm volatile("" ::: "memory"); }
template <int N> DI void barrier_vm() { asm volatile("s_waitcnt vmcnt(%0)" :: "i"(N) : "memory"); waitlgkm<0>(); __builtin_amdgcn_s_barrier(); asm volatile("" ::: "memory"); }
DI u32 cvtpk(float lo, float hi) { f32x2 v = {lo, hi}; bf16x2 b = __builtin_convertvector(v, bf16x2); return __builtin_bit_cast(u32, b); }
DI u32x2 pack4(const f32x4& a, float s) { return u32x2{cvtpk(a[0] * s, a[1] * s), cvtpk(a[2] * s, a[3] * s)}; }
DI u32x2 pack4n(const f32x4& a) { return u32x2{cvtpk(a[0], a[1]), cvtpk(a[2], a[3])}; }
template <class T> DI LAS T* lp(u32 off) { return (LAS T*)off; }
DI bf16x8 lds_b128(LAS char*, u32 off) { return *lp<bf16x8>(off); }
DI void lds_w128(LAS char*, u32 off, bf16x8 v) { *lp<bf16x8>(off) = v; }
DI void lds_w64(LAS char*, u32 off, u32x2 v) { *lp<u32x2>(off) = v; }
DI bf16x8 lds_tr2(LAS char*, u32 off0, u32 off1) {
  bf16x4 a = __builtin_amdgcn_ds_read_tr16_b64_v4bf16(lp<bf16x4>(off0));
  bf16x4 b = __builtin_amdgcn_ds_read_tr16_b64_v4bf16(lp<bf16x4>(off1));
  return __builtin_shufflevector(a, b, 0, 1, 2, 3, 4, 5, 6, 7);
}
#define STAMP(i)
#define STAMPC(i)
#define STAMPOUT()
#define SB() __builtin_amdgcn_sched_barrier(0)
#define TICK(i)
#define TICKDECL
#define TICKOUT
#define PIN(x) asm volatile("" : "+v"(x))
#define OPAQUE_S(x) asm volatile("" : "+s"(x))
DI float vmax3(float a, float b, float c) { return __builtin_elementwise_maximum(__builtin_elementwise_maximum(a, b), c); }
#define MFMA_ACC(acc, a, b) asm volatile("v_mfma_f32_16x16x32_bf16 %0, %1, %2, %0" : "+v"(acc) : "v"(a), "v"(b))
#define MFMA_INIT(acc, a, b, c) asm volatile("v_mfma_f32_16x16x32_bf16 %0, %1, %2, %3" : "=&v"(acc) : "v"(a), "v"(b), "v"(c))
#define MFMA_ZERO(acc, a, b) asm volatile("v_mfma_f32_16x16x32_bf16 %0, %1, %2, 0" : "=&v"(acc) : "v"(a), "v"(b))
#define NOP8() asm volatile("s_nop 7")
#define NOP2() asm volatile("s_nop 1")
#define EXP2(x) __builtin_amdgcn_exp2f(x)
DI int lane_id() { int ln; asm volatile("v_mbcnt_lo_u32_b32 %0, -1, 0\n\tv_mbcnt_hi_u32_b32 %0, -1, %0" : "=v"(ln)); return ln; }
DI float xg_max(float x) {
  auto a = __builtin_amdgcn_permlane16_swap(__float_as_uint(x), __float_as_uint(x), false, false);
  x = fmaxf(__uint_as_float(a[0]), __uint_as_float(a[1]));
  auto b = __builtin_amdgcn_permlane32_swap(__float_as_uint(x), __float_as_uint(x), false, false);
  return fmaxf(__uint_as_float(b[0]), __uint_as_float(b[1]));
}
DI float xg_sum(float x) {
  auto a = __builtin_amdgcn_permlane16_swap(__float_as_uint(x), __float_as_uint(x), false, false);
  x = __uint_as_float(a[0]) + __uint_as_float(a[1]);
  auto b = __builtin_amdgcn_permlane32_swap(__float_as_uint(x), __float_as_uint(x), false, false);
  return __uint_as_float(b[0]) + __uint_as_float(b[1]);
}

struct Ctx {
  LAS char* L; u32 ldsb, wave, w, br, b, hg, j;
  __amdgpu_buffer_rsrc_t srdK, srdV;
};

DI void issue_kv(Ctx& c, const __amdgpu_buffer_rsrc_t srd, u32 st, u32 la, u32 vo0, u32 vo1, u32 vo2, u32 vo3) {
  asm volatile("s_mov_b32 m0, %0\n\ts_nop 0\n\t"
               "buffer_load_dwordx4 %1, %5, %6 offen nt lds\n\t"
               "buffer_load_dwordx4 %2, %5, %6 offen offset:1024 nt lds\n\t"
               "buffer_load_dwordx4 %3, %5, %6 offen offset:2048 nt lds\n\t"
               "buffer_load_dwordx4 %4, %5, %6 offen offset:3072 nt lds"
               :: "s"(rfl(c.ldsb + la + 4096u * c.w)), "v"(vo0), "v"(vo1), "v"(vo2), "v"(vo3), "s"(srd), "s"(rfl(st * SLOT + 4096u * c.w)) : "memory");
}

DI u32 wq_pattern(const Ctx& c, int ln) { return 8192u * (ln >> 3) + 16u * ((ln & 7) ^ (2u * (((ln >> 4) & 1) + 2u * (c.w & 1)))); }
DI void issue_wq(const Ctx& c, const __amdgpu_buffer_rsrc_t srdW, u32 vo_w, u32 rdn, u32 pi, u32 la) {
  const u32 b = c.b, j = c.j; (void)j; (void)b;
  const u32 hd = c.hg * 4 + rdn;
  const u32 kq = ((pi & 3) + KROT) & 3, dh = (pi >> 2) ^ DROT;
  const u32 so = (128u * kq + 8u * c.w) * 8192u + (128u * hd + 64u * dh) * 2u;
  #pragma unroll
  for (int i = 0; i < 4; ++i) dma_w(rfl(c.ldsb + la + 1024u * c.w + 4096u * i), vo_w, srdW, rfl(so + 32u * 8192u * i));
}
DI void issue_x_rows(const Params& p, Ctx& c, int ln) {
  const __amdgpu_buffer_rsrc_t srdX = mksrd((const char*)p.x[c.br] + (size_t)c.b * SQ * DM * 2, SQ * DM * 2);
  const u32 start = 56u * (c.j & 1);
  auto one = [&](u32 row) {
    row += start; row = row >= 112u ? row - 112u : row;
    dma_w(rfl(c.ldsb + 1024u * row), (16u * ln) ^ rfl(16u * (row & 15u)), srdX, rfl(1024u * row));
  };
  if (c.wave < 4) {
    #pragma unroll
    for (int i = 0; i < 16; ++i) one(c.w + 4u * i);
  } else {
    #pragma unroll
    for (int i = 0; i < 12; ++i) one(64u + c.w + 4u * i);
  }
}

template <int NQ> struct Proj {
  bf16x8 xf[NQ][16];
  f32x4 qa[4][NQ];
  u32 wA, rowq, oW;
  DI void init(const Ctx& c, int ln) {
    const u32 cl = ln & 15, g = ln >> 4, q4 = cl >> 2, p4 = ln & 3;
    wA = 128u * (8u * g + q4) + 16u * ((2u * ((q4 >> 1) + 2u * (g & 1))) | (p4 >> 1)) + 8u * (p4 & 1);
    rowq = L_QH + 256u * (32u * c.w + cl);
    oW = 16u * ((g >> 1) ^ cl) + 8u * (g & 1);
  }
  DI void read_x(const Ctx& c, int ln) {
    const u32 b = c.b, j = c.j; (void)j; (void)b;
    const u32 cl = ln & 15, g = ln >> 4;
    const u32 xA = 1024u * (32u * c.w + cl) + 16u * (g ^ cl);
    #pragma unroll
    for (int jq = 0; jq < 4; ++jq) {
      const u32 a = xA + 256u * (((u32)jq + KROT) & 3u);
      #pragma unroll
      for (int i = 0; i < NQ; ++i)
        #pragma unroll
        for (int ks = 0; ks < 4; ++ks) xf[i][4 * jq + ks] = lds_b128(c.L, (a ^ (64u * ks)) + 16384u * i);
    }
  }
  template <int PFV = PF>
  DI void piece(const Ctx& c, const int kq, u32 la, u32 dh) {
    LAS char* L = c.L;
    OPAQUE_S(la);
    u32 wa[4];
    #pragma unroll
    for (int d = 0; d < 4; ++d) wa[d] = la + (wA ^ (32u * d));
    auto wread = [&](const int n) { return lds_tr2(L, wa[n & 3] + 4096u * (n >> 2), wa[n & 3] + 4096u * (n >> 2) + 512u); };
    bf16x8 fr[16];
    #pragma unroll
    for (int n = 0; n < 2 * PFV; ++n) fr[n] = wread(n);
    SB();
    #pragma unroll
    for (int hp = 0; hp < 8; ++hp) {
      const int young = 4 * ((7 - hp) < (PFV - 1) ? (7 - hp) : (PFV - 1));
      if (young >= 12) waitlgkm<12>(); else if (young == 8) waitlgkm<8>(); else if (young == 4) waitlgkm<4>(); else waitlgkm<0>();
      SB();
      #pragma unroll
      for (int e = 0; e < 2; ++e) {
        const int n = 2 * hp + e, ks = n >> 2, d = n & 3;
        #pragma unroll
        for (int i = 0; i < NQ; ++i) { if (kq == 0 && ks == 0) MFMA_ZERO(qa[d][i], fr[n], xf[i][4 * kq + ks]); else MFMA_ACC(qa[d][i], fr[n], xf[i][4 * kq + ks]); }
      }
      SB();
      if (hp + PFV < 8) { fr[2 * (hp + PFV)] = wread(2 * (hp + PFV)); fr[2 * (hp + PFV) + 1] = wread(2 * (hp + PFV) + 1); }
      SB();
    }
    if (kq == 3) {
      NOP8();
      SB();
      #pragma unroll
      for (int i = 0; i < NQ; ++i)
        #pragma unroll
        for (int d = 0; d < 4; ++d) lds_w64(L, rowq + 4096u * i + (oW ^ (32u * (4u * dh + d))), pack4(qa[d][i], QSCALE));
    }
  }
};

template <int NQ>
DI void attention_role(const Params& p, Ctx& c, bf16x8 (&park)[16]) {
  LAS char* L = c.L;
  const u32 w = c.w, wave = c.wave; (void)wave;
  const int ln = lane_id();
  const u32 cl = ln & 15, g = ln >> 4, q4 = cl >> 2, p4 = ln & 3;
  const u32 pi_ = (cl & 3) | ((cl & 4) << 1) | ((cl & 8) >> 1);
  const u32 vk0 = 256u * g + 16u * (cl ^ g), vk1 = vk0 ^ 64u, vk2 = vk0 ^ 128u, vk3 = vk0 ^ 192u;
  const u32 kA = 256u * pi_ + 16u * (g ^ pi_);
  const u32 vA = 256u * (8u * (g & 1) + 4u * (g >> 1) + q4) + 16u * ((2u * (q4 + 4u * (g & 1))) | (p4 >> 1)) + 8u * (p4 & 1);
  const u32 rowq = L_QH + 256u * (32u * w + cl);
  const u32 qA = rowq + 16u * (g ^ cl);
  const u32 oW = 16u * ((g >> 1) ^ cl) + 8u * (g & 1);
  {
    const u32 b = c.b, j = c.j; (void)j; (void)b;
    const __amdgpu_buffer_rsrc_t srdW = mksrd(p.wq[c.br], DM * NH * DH * 2);
    const u32 vo_w = wq_pattern(c, ln);
    const u32 dh1 = DROT ^ 1u;
    Proj<NQ> pj;
    pj.init(c, ln);
    issue_x_rows(p, c, ln); issue_wq(c, srdW, vo_w, 0, 4, 8 * SLOT);
    barrier_vm<4>();
    pj.read_x(c, ln);
    barrier_vm<0>();
    issue_wq(c, srdW, vo_w, 0, 5, 3 * SLOT); issue_wq(c, srdW, vo_w, 0, 6, 4 * SLOT);
    pj.template piece<PFP>(c, 0, 8 * SLOT, dh1);
    barrier_vm<4>();
    issue_wq(c, srdW, vo_w, 0, 7, 2 * SLOT);
    pj.template piece<PFP>(c, 1, 3 * SLOT, dh1);
    barrier_vm<4>();
    issue_kv(c, c.srdK, 0, L_K, vk0, vk1, vk2, vk3);
    pj.template piece<PFP>(c, 2, 4 * SLOT, dh1);
    barrier_vm<4>();
    issue_kv(c, c.srdK, 1, L_K + SLOT, vk0, vk1, vk2, vk3);
    pj.template piece<PFP>(c, 3, 2 * SLOT, dh1);
    barrier_vm<4>();
  }
  issue_kv(c, c.srdK, 2, L_K + 2 * SLOT, vk0, vk1, vk2, vk3);
  bf16x8 kf[16];
  #pragma unroll
  for (int i = 0; i < 16; ++i) kf[i] = lds_b128(L, L_K + (kA ^ (64u * (i & 3))) + 4096u * (i >> 2));
  barrier_vm<4>();
  STAMP(1);
  bf16x8 qf[NQ][4];
  #pragma unroll
  for (int i = 0; i < NQ; ++i)
    #pragma unroll
    for (int ks = 0; ks < 4; ++ks) qf[i][ks] = lds_b128(L, (qA ^ (64u * ks)) + 4096u * i);
  waitlgkm<0>();
  if (NQ == 1) {
    #pragma unroll
    for (int i = 0; i < 16; ++i) park[i] = bf16x8{};
  }
  bf16x8 ones = __builtin_bit_cast(bf16x8, u32x4{0x3F803F80u, 0x3F803F80u, 0x3F803F80u, 0x3F803F80u});
  PIN(ones);
  u32 s3 = 0;
  TICKDECL
  #pragma unroll 1
  for (int rd = 0; rd < 4; ++rd) {
    f32x4 O[8][NQ];
    #pragma unroll
    for (int d = 0; d < 8; ++d)
      #pragma unroll
      for (int i = 0; i < NQ; ++i) O[d][i] = f32x4{};
    float m[NQ];
    f32x4 negm[NQ], l[NQ];
    #pragma unroll
    for (int i = 0; i < NQ; ++i) { m[i] = 0.f; l[i] = f32x4{}; negm[i] = f32x4{}; }
    #pragma unroll 1
    for (int t = 0; t < 8; ++t) {
      const u32 s3n = (s3 == 2) ? 0 : s3 + 1;
      const u32 vbase = L_V + s3 * SLOT, kbase = L_K + s3n * SLOT;
      const u32 sK = 8u * rd + t + 3;
      const bool kdma = sK < 32;
      TICK(0);
      const u32 kdst = L_K + s3 * SLOT;
      if (KPOS == 0 && kdma) issue_kv(c, c.srdK, sK, kdst, vk0, vk1, vk2, vk3);
      TICK(1);
      if (NQ == 1 && rd == 1 && t == 1) {
        #pragma unroll
        for (int i = 0; i < 16; ++i) park[i] = lds_b128(L, ((L_QH + 256u * cl + 16u * (g ^ cl)) ^ (64u * (i & 3))) + 4096u * ((i >> 2) == 3 ? 6 : (i >> 2)));
      }
      u32 va[8];
      va[0] = vbase + vA;
      #pragma unroll
      for (int d = 1; d < 8; ++d) va[d] = va[0] ^ (32u * d);
      auto vread = [&](const int d, const int u) { return lds_tr2(L, va[d] + 8192u * u, va[d] + 8192u * u + 4096u); };
      f32x4 S[4][NQ];
      bf16x8 vf[16];
      #pragma unroll
      for (int kt = 0; kt < 2; ++kt)
        #pragma unroll
        for (int ks = 0; ks < 4; ++ks) {
          #pragma unroll
          for (int i = 0; i < NQ; ++i) { if (ks == 0) MFMA_INIT(S[kt][i], kf[4 * kt + ks], qf[i][ks], negm[i]); else MFMA_ACC(S[kt][i], kf[4 * kt + ks], qf[i][ks]); }
          vf[4 * kt + ks] = vread(4 * kt + ks, 0);
          SB();
        }
      u32x4 pw[NQ][2];
      float pa[NQ];
      #pragma unroll
      for (int kt = 2; kt < 4; ++kt)
        #pragma unroll
        for (int ks = 0; ks < 4; ++ks) {
          const int n = 4 * (kt - 2) + ks;
          #pragma unroll
          for (int i = 0; i < NQ; ++i) { if (ks == 0) MFMA_INIT(S[kt][i], kf[4 * kt + ks], qf[i][ks], negm[i]); else MFMA_ACC(S[kt][i], kf[4 * kt + ks], qf[i][ks]); }
          vf[8 + n] = vread(n, 1);
          if (n >= 2 && n < 6) {
            const int k2 = (n - 2) >> 1, e = 2 * ((n - 2) & 1);
            #pragma unroll
            for (int i = 0; i < NQ; ++i) {
              const float x0 = S[k2][i][e], x1 = S[k2][i][e + 1];
              pa[i] = (n == 2) ? __builtin_elementwise_maximum(x0, x1) : vmax3(pa[i], x0, x1);
              u32 pk = cvtpk(EXP2(x0), EXP2(x1)); PIN(pk);
              pw[i][0][2 * k2 + (e >> 1)] = pk;
            }
          }
          SB();
        }
      asm volatile("" ::: "memory");
      NOP8();
      SB();
      TICK(2);
      bool grow = false;
      #pragma unroll
      for (int i = 0; i < NQ; ++i) {
        const float pb = vmax3(S[2][i][0], S[2][i][1], S[2][i][2]);
        pa[i] = vmax3(pa[i], S[2][i][3], S[3][i][0]); pa[i] = vmax3(pa[i], S[3][i][1], S[3][i][2]);
        pa[i] = vmax3(pa[i], pb, S[3][i][3]);
      }
      grow = (NQ == 2 ? __builtin_elementwise_maximum(pa[0], pa[NQ - 1]) : pa[0]) > THR;
      if (__builtin_expect(t == 0 || __any(grow), 0)) {
        #pragma unroll
        for (int i = 0; i < NQ; ++i) {
          const float pmx = xg_max(pa[i]);
          const float delta = (t == 0) ? pmx : fmaxf(pmx, 0.f);
          const float alpha = (t == 0) ? 0.f : EXP2(-delta);
          m[i] += delta; l[i] *= alpha;
          negm[i] = f32x4{-m[i], -m[i], -m[i], -m[i]};
          #pragma unroll
          for (int kt = 0; kt < 4; ++kt) S[kt][i] -= delta;
          #pragma unroll
          for (int d = 0; d < 8; ++d) O[d][i] *= alpha;
          #pragma unroll
          for (int k2 = 0; k2 < 2; ++k2)
            #pragma unroll
            for (int e = 0; e < 4; e += 2) pw[i][0][2 * k2 + (e >> 1)] = cvtpk(EXP2(S[k2][i][e]), EXP2(S[k2][i][e + 1]));
        }
        SB();
        NOP2();
      }
      if (KPOS == 1 && kdma) issue_kv(c, c.srdK, sK, kdst, vk0, vk1, vk2, vk3);
      waitlgkm<15>();
      SB();
      TICK(3);
      #pragma unroll
      for (int i = 0; i < NQ; ++i) MFMA_ACC(l[i], ones, __builtin_bit_cast(bf16x8, pw[i][0]));
      #pragma unroll
      for (int d = 0; d < 8; ++d) {
        #pragma unroll
        for (int i = 0; i < NQ; ++i) MFMA_ACC(O[d][i], vf[d], __builtin_bit_cast(bf16x8, pw[i][0]));
        if (d < 4) {
          const int k2 = 2 + (d >> 1), e = 2 * (d & 1);
          #pragma unroll
          for (int i = 0; i < NQ; ++i) { u32 pk = cvtpk(EXP2(S[k2][i][e]), EXP2(S[k2][i][e + 1])); PIN(pk); pw[i][1][2 * (k2 - 2) + (e >> 1)] = pk; }
        }
        SB();
      }
      u32 ka[4];
      ka[0] = kbase + kA;
      #pragma unroll
      for (int ks = 1; ks < 4; ++ks) ka[ks] = ka[0] ^ (64u * ks);
      if (KPOS == 2 && kdma) issue_kv(c, c.srdK, sK, kdst, vk0, vk1, vk2, vk3);
      waitlgkm<0>();
      SB();
      #pragma unroll
      for (int i = 0; i < NQ; ++i) MFMA_ACC(l[i], ones, __builtin_bit_cast(bf16x8, pw[i][1]));
      #pragma unroll
      for (int d = 0; d < 8; ++d) {
        #pragma unroll
        for (int i = 0; i < NQ; ++i) MFMA_ACC(O[d][i], vf[8 + d], __builtin_bit_cast(bf16x8, pw[i][1]));
        SB();
        kf[2 * d] = lds_b128(L, ka[(2 * d) & 3] + 4096u * ((2 * d) >> 2));
        kf[2 * d + 1] = lds_b128(L, ka[(2 * d + 1) & 3] + 4096u * ((2 * d + 1) >> 2));
        SB();
      }
      s3 = s3n;
      TICK(4);
      if (kdma) barrier_vm<4>(); else barrier_vm<0>();
      TICK(5);
    }
    if (rd < 3) {
      #pragma unroll
      for (int i = 0; i < NQ; ++i)
        #pragma unroll
        for (int ks = 0; ks < 4; ++ks) qf[i][ks] = lds_b128(L, (qA ^ (64u * ks)) + 4096u * i);
    }
    const u32 orow = rowq - ((rd == 3) ? L_QH : 0u);
    NOP8();
    SB();
    #pragma unroll
    for (int i = 0; i < NQ; ++i) {
      const float inv = 1.0f / l[i][0];
      #pragma unroll
      for (int d = 0; d < 8; ++d) lds_w64(L, orow + 4096u * i + (oW ^ (32u * d)), pack4(O[d][i], inv));
    }
    waitlgkm<0>();
    STAMP(2 + rd);
  }
  TICKOUT
}

template <int NQ>
DI void projection_role(const Params& p, Ctx& c, bf16x8 (&park)[16]) {
  LAS char* L = c.L;
  const u32 w = c.w, wave = c.wave; (void)wave;
  const u32 b = c.b, j = c.j; (void)j;
  const int ln = lane_id();
  const u32 cl = ln & 15, g = ln >> 4, q4 = cl >> 2, p4 = ln & 3;
  const u32 vv0 = 256u * g + 16u * (cl ^ (2u * g)), vv2 = vv0 ^ 128u;
  const __amdgpu_buffer_rsrc_t srdW = mksrd(p.wq[c.br], DM * NH * DH * 2);
  const u32 rot = KROT, dh0 = DROT;
  const u32 vo_w = wq_pattern(c, ln);
  auto issue_w = [&](u32 rdn, u32 pi, u32 la) { issue_wq(c, srdW, vo_w, rdn, pi, la); };
  Proj<NQ> pj;
  pj.init(c, ln);
  const u32 rowq = pj.rowq;
  const u32 qA = rowq + 16u * (g ^ cl);
  STAMP(13);
  issue_w(0, 0, 7 * SLOT); issue_x_rows(p, c, ln); issue_w(0, 1, 9 * SLOT);
  STAMP(14);
  barrier_vm<4>();
  STAMP(1);
  pj.read_x(c, ln);
  barrier_lgkm();
  issue_w(0, 2, 5 * SLOT);
  pj.template piece<PFP>(c, 0, 7 * SLOT, dh0);
  barrier_vm<4>();
  STAMP(2);
  issue_w(0, 3, 6 * SLOT);
  pj.template piece<PFP>(c, 1, 9 * SLOT, dh0);
  barrier_vm<4>();
  issue_w(1, 0, 7 * SLOT);
  pj.template piece<PFP>(c, 2, 5 * SLOT, dh0);
  barrier_vm<4>();
  STAMP(3);
  issue_kv(c, c.srdV, 0, L_V, vv0, vv0, vv2, vv2);
  pj.template piece<PFP>(c, 3, 6 * SLOT, dh0);
  issue_kv(c, c.srdV, 1, L_V + SLOT, vv0, vv0, vv2, vv2);
  barrier_lgkm();
  STAMP(4);
  barrier_vm<4>();
  STAMP(5);
  bf16x8 og1[NQ][4];
  #pragma unroll
  for (int i = 0; i < NQ; ++i)
    #pragma unroll
    for (int ks = 0; ks < 4; ++ks) og1[i][ks] = bf16x8{};
  if (NQ == 1) {
    #pragma unroll
    for (int i = 4; i < 16; ++i) park[i] = bf16x8{};
  }
  u32 s3 = 0;
  TICKDECL
  #pragma unroll 1
  for (int hh = 0; hh < 6; ++hh) {
    #pragma unroll
    for (int kq = 0; kq < 4; ++kq) {
      const u32 s = 4u * hh + kq, s1 = s + 1;
      const u32 vdst = L_V + ((s3 == 0) ? 2 : s3 - 1) * SLOT;
      TICK(0);
      issue_w((1 + (s1 >> 3)) & 3, s1 & 7, L_W + (kq & 1) * SLOT);
      TICK(1);
      if (NQ == 1 && kq == 1 && hh == 2) {
        #pragma unroll
        for (int i = 4; i < 16; ++i) park[i] = lds_b128(L, ((L_QH + 256u * cl + 16u * (g ^ cl)) ^ (64u * (i & 3))) + 4096u * (2 + (i >> 2)));
      }
      if (kq == 1 && hh == 4) {
        #pragma unroll
        for (int i = 0; i < NQ; ++i)
          #pragma unroll
          for (int ks = 0; ks < 4; ++ks) og1[i][ks] = lds_b128(L, (qA ^ (64u * ks)) + 4096u * i);
      }
      pj.piece(c, kq, L_W + ((kq + 1) & 1) * SLOT, (u32)(hh & 1) ^ dh0);
      issue_kv(c, c.srdV, s + 2, vdst, vv0, vv0, vv2, vv2);
      s3 = (s3 == 2) ? 0 : s3 + 1;
      TICK(2);
      barrier_vm<4>();
      TICK(4);
    }
  }
  #pragma unroll
  for (int i = 0; i < 4 * NQ; ++i) park[i] = bf16x8{};
  const __amdgpu_buffer_rsrc_t srdO = mksrd(p.wo[c.br], NH * DH * DM * 2);
  #pragma unroll 1
  for (u32 s = 24; s < 32; ++s) {
    const u32 vdst = L_V + ((s3 == 0) ? 2 : s3 - 1) * SLOT;
    if (s + 2 < 32) issue_kv(c, c.srdV, s + 2, vdst, vv0, vv0, vv2, vv2);
    dma_w(rfl(c.ldsb + L_W + 1024u * w), 16u * ln, srdO, rfl((c.hg * 512u + 32u * b + 4u * (s - 24) + w) * 1024u));
    if (s == 25) {
      #pragma unroll
      for (int i = 0; i < NQ; ++i)
        #pragma unroll
        for (int ks = 0; ks < 4; ++ks) park[4 * i + ks] = lds_b128(L, (qA ^ (64u * ks)) + 4096u * i);
    }
    s3 = (s3 == 2) ? 0 : s3 + 1;
    if (s < 30) barrier_vm<5>(); else barrier_vm<0>();
  }
  STAMP(6);
  TICKOUT
  #pragma unroll
  for (int i = 0; i < NQ; ++i)
    #pragma unroll
    for (int ks = 0; ks < 4; ++ks) lds_w128(L, OREG + ((qA - L_QH) ^ (64u * ks)) + 4096u * i, og1[i][ks]);
}

__global__ __launch_bounds__(512, 2) void fused_attn(Params p) {
  extern __shared__ __attribute__((aligned(16))) char smem[];
  Ctx c;
  c.L = (LAS char*)smem;
  c.ldsb = (u32)(size_t)smem;
  c.wave = rfl(threadIdx.x >> 6);
  c.w = c.wave & 3;
  const u32 wave = c.wave, w = c.w;
  const u32 bid = blockIdx.x, xcd = bid & 7;
  c.j = bid >> 3;
  c.br = xcd & 1; c.hg = 2 * (xcd >> 1) + (c.j & 1); c.b = c.j >> 1;
  const size_t kvoff = (size_t)((c.b * NH + c.hg * 4) * SK) * DH * 2;
  c.srdK = mksrd((const char*)p.k[c.br] + kvoff, 4 * SK * DH * 2);
  c.srdV = mksrd((const char*)p.v[c.br] + kvoff, 4 * SK * DH * 2);
  STAMP(0); STAMPC(8);
  bf16x8 park[16];
  if (wave < 3) attention_role<2>(p, c, park);
  else if (wave == 3) attention_role<1>(p, c, park);
  else if (wave < 7) projection_role<2>(p, c, park);
  else projection_role<1>(p, c, park);

  {
    LAS char* L = c.L;
    const int ln = lane_id();
    const u32 cl = ln & 15, g = ln >> 4, q4 = cl >> 2, p4 = ln & 3;
    const __amdgpu_buffer_rsrc_t srdO = mksrd(p.wo[c.br], NH * DH * DM * 2);
    const u32 vo0 = 1024u * (ln >> 3) + 16u * ((ln & 7) ^ (2u * ((ln >> 4) & 1))), vo1 = 1024u * (ln >> 3) + 16u * ((ln & 7) ^ (2u * (((ln >> 4) & 1) + 2u)));
    const u32 ring = L_RING + wave * (3 * PIECE);
    auto issue_o = [&](u32 u, u32 slot3) {
      const u32 hs = (0x2013u >> (4u * ((u >> 2) & 3))) & 3u;
      const u32 so = (u < 16) ? ((c.hg * 4 + hs) * 128u + 32u * (u & 3)) * 1024u + 128u * wave : 0x01000000u;
      const u32 lb = c.ldsb + ring + slot3 * PIECE;
      dma_wo(rfl(lb), vo0, srdO, rfl(so));                     dma_wo(rfl(lb + 1024u), vo1, srdO, rfl(so + 8192u));
      dma_wo(rfl(lb + 2048u), vo0, srdO, rfl(so + 16384u));     dma_wo(rfl(lb + 3072u), vo1, srdO, rfl(so + 24576u));
    };
    issue_o(0, 0); issue_o(1, 1); issue_o(2, 2);
    barrier_lgkm();
    STAMP(7);
    const u32 oA = 256u * cl + 16u * (g ^ cl);
    const u32 wA = 128u * (8u * g + q4) + 16u * ((2u * ((q4 >> 1) + 2u * (g & 1))) | (p4 >> 1)) + 8u * (p4 & 1);
    auto aread = [&](bf16x8 (&af)[4], u32 slot3) {
      const u32 a0 = ring + slot3 * PIECE + wA;
      #pragma unroll
      for (int nt = 0; nt < 4; ++nt) { const u32 a = a0 ^ (32u * nt); af[nt] = lds_tr2(L, a, a + 512u); }
    };
    f32x4 acc[4][7];
    bf16x8 afA[4], afB[4], bq[7];
    auto oaddr = [&](u32 u) { return OREG * ((u >> 2) & 1) + (oA ^ (64u * (u & 3))); };
    SB();
    waitvm<8>();
    aread(afA, 0);
    {
      const u32 ob = oaddr(0);
      #pragma unroll
      for (int qt = 0; qt < 4; ++qt) bq[qt] = lds_b128(L, ob + 4096u * qt);
    }
    u32 sl = 0;
    auto kstep = [&](const u32 u, bf16x8 (&af)[4], bf16x8 (&afn)[4]) {
      const u32 sl1 = (sl == 2) ? 0 : sl + 1;
      const u32 ob = oaddr(u), obn = oaddr(u + 1);
      const bool pre = (u & 7) != 7;
      #pragma unroll
      for (int qt = 0; qt < 7; ++qt) {
        #pragma unroll
        for (int nt = 0; nt < 4; ++nt) { if (u == 0) MFMA_ZERO(acc[nt][qt], af[nt], bq[qt]); else MFMA_ACC(acc[nt][qt], af[nt], bq[qt]); }
        SB();
        if (qt < 3) bq[qt + 4] = lds_b128(L, ob + 4096u * (qt + 4));
        else if (pre) bq[qt - 3] = lds_b128(L, obn + 4096u * (qt - 3));
        if (qt == 0) issue_o(u + 3, sl);
        if (qt == 2) { waitvm<8>(); aread(afn, sl1); }
        SB();
      }
      sl = sl1;
    };
    #pragma unroll
    for (u32 u = 0; u < 16; u += 2) {
      if (u == 8) {
        barrier_lgkm();
        if (wave == 3 || wave == 7) {
          #pragma unroll
          for (int i = 0; i < 16; ++i) {
            if (wave == 3) lds_w128(L, (oA ^ (64u * (i & 3))) + 4096u * ((i >> 2) == 3 ? 6 : (i >> 2)), park[i]);
            else if (i >= 4) lds_w128(L, (oA ^ (64u * (i & 3))) + 4096u * (2 + (i >> 2)), park[i]);
          }
        }
        if (wave >= 4) {
          #pragma unroll
          for (int ks = 0; ks < 4; ++ks) {
            lds_w128(L, OREG + (oA ^ (64u * ks)) + 8192u * w, park[ks]);
            if (w < 3) lds_w128(L, OREG + (oA ^ (64u * ks)) + 8192u * w + 4096u, park[4 + ks]);
          }
        }
        barrier_lgkm();
        const u32 ob = oaddr(8);
        #pragma unroll
        for (int qt = 0; qt < 4; ++qt) bq[qt] = lds_b128(L, ob + 4096u * qt);
      }
      kstep(u, afA, afB);
      kstep(u + 1, afB, afA);
    }
    waitvm<0>();
    STAMP(11);
    NOP8();
    SB();
    char* part = (char*)p.ws + WS_P + (size_t)((c.br * NB + c.b) * 8 + c.hg) * PART_WG + wave * 14336u + ln * 16;
    #pragma unroll
    for (int qt = 0; qt < 7; ++qt)
      #pragma unroll
      for (int np = 0; np < 2; ++np) {
        const u32x2 a = pack4n(acc[2 * np][qt]), bb = pack4n(acc[2 * np + 1][qt]);
        __builtin_nontemporal_store(u32x4{a[0], a[1], bb[0], bb[1]}, (u32x4*)(part + (qt * 2 + np) * 1024));
      }
  }
  STAMP(10); STAMPC(9);
  STAMPOUT();
}

__global__ __launch_bounds__(128) void reduce_out(Params p) {
  __shared__ __attribute__((aligned(16))) char tile[16 * 128];
  const int tid = threadIdx.x, ln = tid & 63, np = tid >> 6;
  const u32 blk = blockIdx.x;
  const u32 brb = blk / 56, rem = blk % 56, v = rem / 7, qt = rem % 7;
  const char* src = (const char*)p.ws + WS_P + (size_t)brb * 8 * PART_WG + v * 14336u + (qt * 2 + np) * 1024 + ln * 16;
  u32x4 x[8];
  #pragma unroll
  for (int gq = 0; gq < 8; ++gq) x[gq] = __builtin_nontemporal_load((const u32x4*)(src + (size_t)gq * PART_WG));
  float a[8] = {};
  #pragma unroll
  for (int gq = 0; gq < 8; ++gq)
    #pragma unroll
    for (int i = 0; i < 4; ++i) { a[2 * i] += __uint_as_float(x[gq][i] << 16); a[2 * i + 1] += __uint_as_float(x[gq][i] & 0xffff0000u); }
  const u32 cl = ln & 15, g = ln >> 4;
  *(u32x2*)(tile + cl * 128 + 64 * np + 8 * g) = u32x2{cvtpk(a[0], a[1]), cvtpk(a[2], a[3])};
  *(u32x2*)(tile + cl * 128 + 64 * np + 32 + 8 * g) = u32x2{cvtpk(a[4], a[5]), cvtpk(a[6], a[7])};
  __syncthreads();
  const u32 row = tid >> 3, ch = tid & 7, br = brb >> 4, b = brb & 15;
  char* out = (char*)p.out + (((size_t)b * SQ + 16 * qt + row) * (2 * DM) + br * DM + 64 * v) * 2 + 16 * ch;
  *(u32x4*)out = *(const u32x4*)(tile + row * 128 + 16 * ch);
}

extern "C" void kernel_launch(void* const* d_in, const int* in_sizes, int n_in, void* d_out, int out_size, void* d_ws, size_t ws_size,
                              hipStream_t stream) {
  Params p;
  for (int i = 0; i < 2; ++i) { p.x[i] = d_in[5 * i]; p.k[i] = d_in[5 * i + 1]; p.v[i] = d_in[5 * i + 2]; p.wq[i] = d_in[5 * i + 3]; p.wo[i] = d_in[5 * i + 4]; }
  p.out = d_out; p.ws = d_ws;
  (void)hipFuncSetAttribute((const void*)fused_attn, hipFuncAttributeMaxDynamicSharedMemorySize, LDS_BYTES);
  fused_attn<<<256, 512, LDS_BYTES, stream>>>(p);
  reduce_out<<<32 * 56, 128, 0, stream>>>(p);
}
```

```cpp
#define PFP 8
#include <hip/hip_runtime.h>
#include <hip/hip_bf16.h>

typedef unsigned u32;
typedef unsigned long long u64;
typedef __bf16 bf16x8 __attribute__((ext_vector_type(8)));
typedef __bf16 bf16x4 __attribute__((ext_vector_type(4)));
typedef __bf16 bf16x2 __attribute__((ext_vector_type(2)));
typedef float f32x4 __attribute__((ext_vector_type(4)));
typedef float f32x2 __attribute__((ext_vector_type(2)));
typedef u32 u32x4 __attribute__((ext_vector_type(4)));
typedef u32 u32x2 __attribute__((ext_vector_type(2)));
#define LAS __attribute__((address_space(3)))
#define DI __device__ __forceinline__

constexpr int NB = 16, SQ = 112, DM = 512, NH = 32, SK = 512, DH = 128;
constexpr float QSCALE = 0.12751743082f;
constexpr float THR = 8.0f;

constexpr u32 SLOT = 16384;
constexpr u32 L_K = 0;
constexpr u32 L_V = 3 * SLOT;
constexpr u32 L_W = 6 * SLOT;
constexpr u32 L_QH = 8 * SLOT;
constexpr u32 LDS_BYTES = 10 * SLOT;
constexpr u32 OREG = 28672;
constexpr u32 L_RING = 2 * OREG;
constexpr u32 PIECE = 4096;

constexpr size_t WS_P = 0;
constexpr u32 PART_WG = 8 * 7 * 2 * 1024;
constexpr size_t WS_DONE = 32u << 20, WS_ACK = WS_DONE + 65536;
constexpr u32 L_FLAG = 159744;
constexpr unsigned long long ACK_MIX = 0x9E3779B97F4A7C15ull;
constexpr u32 SPIN_CAP = 1u << 18;

struct Params {
  const void* x[2]; const void* k[2]; const void* v[2]; const void* wq[2]; const void* wo[2];
  void* out; void* ws;
};

DI u32 rfl(u32 x) { return __builtin_amdgcn_readfirstlane(x); }
DI __amdgpu_buffer_rsrc_t mksrd(const void* p, u32 bytes) { return __builtin_amdgcn_make_buffer_rsrc((void*)p, (short)0, (int)bytes, 0x00020000); }
DI void dma_nt(u32 m0v, u32 voff, __amdgpu_buffer_rsrc_t srd, u32 soff) {
  asm volatile("s_mov_b32 m0, %0\n\ts_nop 0\n\tbuffer_load_dwordx4 %1, %2, %3 offen nt lds" :: "s"(m0v), "v"(voff), "s"(srd), "s"(soff) : "memory");
}
#define WPOL0 ""
#define WPOL1 ""
DI void dma_wo(u32 m0v, u32 voff, __amdgpu_buffer_rsrc_t srd, u32 soff) {
  asm volatile("s_mov_b32 m0, %0\n\ts_nop 0\n\tbuffer_load_dwordx4 %1, %2, %3 offen" WPOL1 " lds" :: "s"(m0v), "v"(voff), "s"(srd), "s"(soff) : "memory");
}
DI void dma_w(u32 m0v, u32 voff, __amdgpu_buffer_rsrc_t srd, u32 soff) {
  asm volatile("s_mov_b32 m0, %0\n\ts_nop 0\n\tbuffer_load_dwordx4 %1, %2, %3 offen" WPOL0 " lds" :: "s"(m0v), "v"(voff), "s"(srd), "s"(soff) : "memory");
}
#define KROTM 1
#define PF 3
#define KPOS 1
#define KROT (KROTM == 1 ? (((b >> 2) + 2 * (j & 1)) & 3) : 0)
#define DROT (KROTM == 1 ? ((b >> 1) & 1) : 0)

template <int N> DI void waitvm() { asm volatile("s_waitcnt vmcnt(%0)" :: "i"(N) : "memory"); }
template <int N> DI void waitlgkm() { __builtin_amdgcn_s_waitcnt(0xC07F | (N << 8)); }
DI void barrier_lgkm() { asm volatile("" ::: "memory"); waitlgkm<0>(); __builtin_amdgcn_s_barrier(); asm volatile("" ::: "memory"); }
template <int N> DI void barrier_vm() { asm volatile("s_waitcnt vmcnt(%0)" :: "i"(N) : "memory"); waitlgkm<0>(); __builtin_amdgcn_s_barrier(); asm volatile("" ::: "memory"); }
DI u32 cvtpk(float lo, float hi) { f32x2 v = {lo, hi}; bf16x2 b = __builtin_convertvector(v, bf16x2); return __builtin_bit_cast(u32, b); }
DI u32x2 pack4(const f32x4& a, float s) { return u32x2{cvtpk(a[0] * s, a[1] * s), cvtpk(a[2] * s, a[3] * s)}; }
DI u32x2 pack4n(const f32x4& a) { return u32x2{cvtpk(a[0], a[1]), cvtpk(a[2], a[3])}; }
template <class T> DI LAS T* lp(u32 off) { return (LAS T*)off; }
DI bf16x8 lds_b128(LAS char*, u32 off) { return *lp<bf16x8>(off); }
DI void lds_w128(LAS char*, u32 off, bf16x8 v) { *lp<bf16x8>(off) = v; }
DI void lds_w64(LAS char*, u32 off, u32x2 v) { *lp<u32x2>(off) = v; }
DI bf16x8 lds_tr2(LAS char*, u32 off0, u32 off1) {
  bf16x4 a = __builtin_amdgcn_ds_read_tr16_b64_v4bf16(lp<bf16x4>(off0));
  bf16x4 b = __builtin_amdgcn_ds_read_tr16_b64_v4bf16(lp<bf16x4>(off1));
  return __builtin_shufflevector(a, b, 0, 1, 2, 3, 4, 5, 6, 7);
}
#define STAMP(i)
#define STAMPC(i)
#define STAMPOUT()
#define SB() __builtin_amdgcn_sched_barrier(0)
#define TICK(i)
#define TICKDECL
#define TICKOUT
#define PIN(x) asm volatile("" : "+v"(x))
#define OPAQUE_S(x) asm volatile("" : "+s"(x))
DI float vmax3(float a, float b, float c) { return __builtin_elementwise_maximum(__builtin_elementwise_maximum(a, b), c); }
#define MFMA_ACC(acc, a, b) asm volatile("v_mfma_f32_16x16x32_bf16 %0, %1, %2, %0" : "+v"(acc) : "v"(a), "v"(b))
#define MFMA_INIT(acc, a, b, c) asm volatile("v_mfma_f32_16x16x32_bf16 %0, %1, %2, %3" : "=&v"(acc) : "v"(a), "v"(b), "v"(c))
#define MFMA_ZERO(acc, a, b) asm volatile("v_mfma_f32_16x16x32_bf16 %0, %1, %2, 0" : "=&v"(acc) : "v"(a), "v"(b))
#define NOP8() asm volatile("s_nop 7")
#define NOP2() asm volatile("s_nop 1")
#define EXP2(x) __builtin_amdgcn_exp2f(x)
#define GAS __attribute__((address_space(1)))
DI u64 ld_agent(const char* p) { return __hip_atomic_load((const GAS u64*)p, __ATOMIC_RELAXED, __HIP_MEMORY_SCOPE_AGENT); }
DI void st_agent(char* p, u64 v) { __hip_atomic_store((GAS u64*)p, v, __ATOMIC_RELAXED, __HIP_MEMORY_SCOPE_AGENT); }
DI int lane_id() { int ln; asm volatile("v_mbcnt_lo_u32_b32 %0, -1, 0\n\tv_mbcnt_hi_u32_b32 %0, -1, %0" : "=v"(ln)); return ln; }
DI float xg_max(float x) {
  auto a = __builtin_amdgcn_permlane16_swap(__float_as_uint(x), __float_as_uint(x), false, false);
  x = fmaxf(__uint_as_float(a[0]), __uint_as_float(a[1]));
  auto b = __builtin_amdgcn_permlane32_swap(__float_as_uint(x), __float_as_uint(x), false, false);
  return fmaxf(__uint_as_float(b[0]), __uint_as_float(b[1]));
}
DI float xg_sum(float x) {
  auto a = __builtin_amdgcn_permlane16_swap(__float_as_uint(x), __float_as_uint(x), false, false);
  x = __uint_as_float(a[0]) + __uint_as_float(a[1]);
  auto b = __builtin_amdgcn_permlane32_swap(__float_as_uint(x), __float_as_uint(x), false, false);
  return __uint_as_float(b[0]) + __uint_as_float(b[1]);
}

struct Ctx {
  LAS char* L; u32 ldsb, wave, w, br, b, hg, j, bid;
  __amdgpu_buffer_rsrc_t srdK, srdV;
};

DI u32 member_bid(const Ctx& c, u32 m) { return 8u * (2u * c.b + (m & 1u)) + c.br + 2u * (m >> 1); }

DI void issue_kv(Ctx& c, const __amdgpu_buffer_rsrc_t srd, u32 st, u32 la, u32 vo0, u32 vo1, u32 vo2, u32 vo3) {
  asm volatile("s_mov_b32 m0, %0\n\ts_nop 0\n\t"
               "buffer_load_dwordx4 %1, %5, %6 offen nt lds\n\t"
               "buffer_load_dwordx4 %2, %5, %6 offen offset:1024 nt lds\n\t"
               "buffer_load_dwordx4 %3, %5, %6 offen offset:2048 nt lds\n\t"
               "buffer_load_dwordx4 %4, %5, %6 offen offset:3072 nt lds"
               :: "s"(rfl(c.ldsb + la + 4096u * c.w)), "v"(vo0), "v"(vo1), "v"(vo2), "v"(vo3), "s"(srd), "s"(rfl(st * SLOT + 4096u * c.w)) : "memory");
}

DI u32 wq_pattern(const Ctx& c, int ln) { return 8192u * (ln >> 3) + 16u * ((ln & 7) ^ (2u * (((ln >> 4) & 1) + 2u * (c.w & 1)))); }
DI void issue_wq(const Ctx& c, const __amdgpu_buffer_rsrc_t srdW, u32 vo_w, u32 rdn, u32 pi, u32 la) {
  const u32 b = c.b, j = c.j; (void)j; (void)b;
  const u32 hd = c.hg * 4 + rdn;
  const u32 kq = ((pi & 3) + KROT) & 3, dh = (pi >> 2) ^ DROT;
  const u32 so = (128u * kq + 8u * c.w) * 8192u + (128u * hd + 64u * dh) * 2u;
  #pragma unroll
  for (int i = 0; i < 4; ++i) dma_w(rfl(c.ldsb + la + 1024u * c.w + 4096u * i), vo_w, srdW, rfl(so + 32u * 8192u * i));
}
DI void issue_x_rows(const Params& p, Ctx& c, int ln) {
  const __amdgpu_buffer_rsrc_t srdX = mksrd((const char*)p.x[c.br] + (size_t)c.b * SQ * DM * 2, SQ * DM * 2);
  const u32 start = 56u * (c.j & 1);
  auto one = [&](u32 row) {
    row += start; row = row >= 112u ? row - 112u : row;
    dma_w(rfl(c.ldsb + 1024u * row), (16u * ln) ^ rfl(16u * (row & 15u)), srdX, rfl(1024u * row));
  };
  if (c.wave < 4) {
    #pragma unroll
    for (int i = 0; i < 16; ++i) one(c.w + 4u * i);
  } else {
    #pragma unroll
    for (int i = 0; i < 12; ++i) one(64u + c.w + 4u * i);
  }
}

template <int NQ> struct Proj {
  bf16x8 xf[NQ][16];
  f32x4 qa[4][NQ];
  u32 wA, rowq, oW;
  DI void init(const Ctx& c, int ln) {
    const u32 cl = ln & 15, g = ln >> 4, q4 = cl >> 2, p4 = ln & 3;
    wA = 128u * (8u * g + q4) + 16u * ((2u * ((q4 >> 1) + 2u * (g & 1))) | (p4 >> 1)) + 8u * (p4 & 1);
    rowq = L_QH + 256u * (32u * c.w + cl);
    oW = 16u * ((g >> 1) ^ cl) + 8u * (g & 1);
  }
  DI void read_x(const Ctx& c, int ln) {
    const u32 b = c.b, j = c.j; (void)j; (void)b;
    const u32 cl = ln & 15, g = ln >> 4;
    const u32 xA = 1024u * (32u * c.w + cl) + 16u * (g ^ cl);
    #pragma unroll
    for (int jq = 0; jq < 4; ++jq) {
      const u32 a = xA + 256u * (((u32)jq + KROT) & 3u);
      #pragma unroll
      for (int i = 0; i < NQ; ++i)
        #pragma unroll
        for (int ks = 0; ks < 4; ++ks) xf[i][4 * jq + ks] = lds_b128(c.L, (a ^ (64u * ks)) + 16384u * i);
    }
  }
  template <int PFV = PF>
  DI void piece(const Ctx& c, const int kq, u32 la, u32 dh) {
    LAS char* L = c.L;
    OPAQUE_S(la);
    u32 wa[4];
    #pragma unroll
    for (int d = 0; d < 4; ++d) wa[d] = la + (wA ^ (32u * d));
    auto wread = [&](const int n) { return lds_tr2(L, wa[n & 3] + 4096u * (n >> 2), wa[n & 3] + 4096u * (n >> 2) + 512u); };
    bf16x8 fr[16];
    #pragma unroll
    for (int n = 0; n < 2 * PFV; ++n) fr[n] = wread(n);
    SB();
    #pragma unroll
    for (int hp = 0; hp < 8; ++hp) {
      const int young = 4 * ((7 - hp) < (PFV - 1) ? (7 - hp) : (PFV - 1));
      if (young >= 12) waitlgkm<12>(); else if (young == 8) waitlgkm<8>(); else if (young == 4) waitlgkm<4>(); else waitlgkm<0>();
      SB();
      #pragma unroll
      for (int e = 0; e < 2; ++e) {
        const int n = 2 * hp + e, ks = n >> 2, d = n & 3;
        #pragma unroll
        for (int i = 0; i < NQ; ++i) { if (kq == 0 && ks == 0) MFMA_ZERO(qa[d][i], fr[n], xf[i][4 * kq + ks]); else MFMA_ACC(qa[d][i], fr[n], xf[i][4 * kq + ks]); }
      }
      SB();
      if (hp + PFV < 8) { fr[2 * (hp + PFV)] = wread(2 * (hp + PFV)); fr[2 * (hp + PFV) + 1] = wread(2 * (hp + PFV) + 1); }
      SB();
    }
    if (kq == 3) {
      NOP8();
      SB();
      #pragma unroll
      for (int i = 0; i < NQ; ++i)
        #pragma unroll
        for (int d = 0; d < 4; ++d) lds_w64(L, rowq + 4096u * i + (oW ^ (32u * (4u * dh + d))), pack4(qa[d][i], QSCALE));
    }
  }
};

template <int NQ>
DI void attention_role(const Params& p, Ctx& c, bf16x8 (&park)[16]) {
  LAS char* L = c.L;
  const u32 w = c.w, wave = c.wave; (void)wave;
  const int ln = lane_id();
  const u32 cl = ln & 15, g = ln >> 4, q4 = cl >> 2, p4 = ln & 3;
  const u32 pi_ = (cl & 3) | ((cl & 4) << 1) | ((cl & 8) >> 1);
  const u32 vk0 = 256u * g + 16u * (cl ^ g), vk1 = vk0 ^ 64u, vk2 = vk0 ^ 128u, vk3 = vk0 ^ 192u;
  const u32 kA = 256u * pi_ + 16u * (g ^ pi_);
  const u32 vA = 256u * (8u * (g & 1) + 4u * (g >> 1) + q4) + 16u * ((2u * (q4 + 4u * (g & 1))) | (p4 >> 1)) + 8u * (p4 & 1);
  const u32 rowq = L_QH + 256u * (32u * w + cl);
  const u32 qA = rowq + 16u * (g ^ cl);
  const u32 oW = 16u * ((g >> 1) ^ cl) + 8u * (g & 1);
  u64 d0 = 0;
  {
    const u32 b = c.b, j = c.j; (void)j; (void)b;
    const __amdgpu_buffer_rsrc_t srdW = mksrd(p.wq[c.br], DM * NH * DH * 2);
    const u32 vo_w = wq_pattern(c, ln);
    const u32 dh1 = DROT ^ 1u;
    Proj<NQ> pj;
    pj.init(c, ln);
    if (NQ == 1) d0 = ld_agent((const char*)p.ws + WS_DONE + 128u * member_bid(c, ln & 7));
    issue_x_rows(p, c, ln); issue_wq(c, srdW, vo_w, 0, 4, 8 * SLOT);
    barrier_vm<4>();
    if (NQ == 1) PIN(d0);
    pj.read_x(c, ln);
    barrier_vm<0>();
    issue_wq(c, srdW, vo_w, 0, 5, 3 * SLOT); issue_wq(c, srdW, vo_w, 0, 6, 4 * SLOT);
    pj.template piece<PFP>(c, 0, 8 * SLOT, dh1);
    barrier_vm<4>();
    issue_wq(c, srdW, vo_w, 0, 7, 2 * SLOT);
    pj.template piece<PFP>(c, 1, 3 * SLOT, dh1);
    barrier_vm<4>();
    issue_kv(c, c.srdK, 0, L_K, vk0, vk1, vk2, vk3);
    pj.template piece<PFP>(c, 2, 4 * SLOT, dh1);
    barrier_vm<4>();
    issue_kv(c, c.srdK, 1, L_K + SLOT, vk0, vk1, vk2, vk3);
    pj.template piece<PFP>(c, 3, 2 * SLOT, dh1);
    barrier_vm<4>();
  }
  issue_kv(c, c.srdK, 2, L_K + 2 * SLOT, vk0, vk1, vk2, vk3);
  bf16x8 kf[16];
  #pragma unroll
  for (int i = 0; i < 16; ++i) kf[i] = lds_b128(L, L_K + (kA ^ (64u * (i & 3))) + 4096u * (i >> 2));
  barrier_vm<4>();
  STAMP(1);
  bf16x8 qf[NQ][4];
  #pragma unroll
  for (int i = 0; i < NQ; ++i)
    #pragma unroll
    for (int ks = 0; ks < 4; ++ks) qf[i][ks] = lds_b128(L, (qA ^ (64u * ks)) + 4096u * i);
  waitlgkm<0>();
  if (NQ == 1) {
    #pragma unroll
    for (int i = 0; i < 16; ++i) park[i] = bf16x8{};
  }
  bf16x8 ones = __builtin_bit_cast(bf16x8, u32x4{0x3F803F80u, 0x3F803F80u, 0x3F803F80u, 0x3F803F80u});
  PIN(ones);
  u32 s3 = 0;
  TICKDECL
  #pragma unroll 1
  for (int rd = 0; rd < 4; ++rd) {
    f32x4 O[8][NQ];
    #pragma unroll
    for (int d = 0; d < 8; ++d)
      #pragma unroll
      for (int i = 0; i < NQ; ++i) O[d][i] = f32x4{};
    float m[NQ];
    f32x4 negm[NQ], l[NQ];
    #pragma unroll
    for (int i = 0; i < NQ; ++i) { m[i] = 0.f; l[i] = f32x4{}; negm[i] = f32x4{}; }
    #pragma unroll 1
    for (int t = 0; t < 8; ++t) {
      const u32 s3n = (s3 == 2) ? 0 : s3 + 1;
      const u32 vbase = L_V + s3 * SLOT, kbase = L_K + s3n * SLOT;
      const u32 sK = 8u * rd + t + 3;
      const bool kdma = sK < 32;
      TICK(0);
      const u32 kdst = L_K + s3 * SLOT;
      if (KPOS == 0 && kdma) issue_kv(c, c.srdK, sK, kdst, vk0, vk1, vk2, vk3);
      TICK(1);
      if (NQ == 1 && rd == 1 && t == 1) {
        #pragma unroll
        for (int i = 0; i < 16; ++i) park[i] = lds_b128(L, ((L_QH + 256u * cl + 16u * (g ^ cl)) ^ (64u * (i & 3))) + 4096u * ((i >> 2) == 3 ? 6 : (i >> 2)));
      }
      u32 va[8];
      va[0] = vbase + vA;
      #pragma unroll
      for (int d = 1; d < 8; ++d) va[d] = va[0] ^ (32u * d);
      auto vread = [&](const int d, const int u) { return lds_tr2(L, va[d] + 8192u * u, va[d] + 8192u * u + 4096u); };
      f32x4 S[4][NQ];
      bf16x8 vf[16];
      #pragma unroll
      for (int kt = 0; kt < 2; ++kt)
        #pragma unroll
        for (int ks = 0; ks < 4; ++ks) {
          #pragma unroll
          for (int i = 0; i < NQ; ++i) { if (ks == 0) MFMA_INIT(S[kt][i], kf[4 * kt + ks], qf[i][ks], negm[i]); else MFMA_ACC(S[kt][i], kf[4 * kt + ks], qf[i][ks]); }
          vf[4 * kt + ks] = vread(4 * kt + ks, 0);
          SB();
        }
      u32x4 pw[NQ][2];
      float pa[NQ];
      #pragma unroll
      for (int kt = 2; kt < 4; ++kt)
        #pragma unroll
        for (int ks = 0; ks < 4; ++ks) {
          const int n = 4 * (kt - 2) + ks;
          #pragma unroll
          for (int i = 0; i < NQ; ++i) { if (ks == 0) MFMA_INIT(S[kt][i], kf[4 * kt + ks], qf[i][ks], negm[i]); else MFMA_ACC(S[kt][i], kf[4 * kt + ks], qf[i][ks]); }
          vf[8 + n] = vread(n, 1);
          if (n >= 2 && n < 6) {
            const int k2 = (n - 2) >> 1, e = 2 * ((n - 2) & 1);
            #pragma unroll
            for (int i = 0; i < NQ; ++i) {
              const float x0 = S[k2][i][e], x1 = S[k2][i][e + 1];
              pa[i] = (n == 2) ? __builtin_elementwise_maximum(x0, x1) : vmax3(pa[i], x0, x1);
              u32 pk = cvtpk(EXP2(x0), EXP2(x1)); PIN(pk);
              pw[i][0][2 * k2 + (e >> 1)] = pk;
            }
          }
          SB();
        }
      asm volatile("" ::: "memory");
      NOP8();
      SB();
      TICK(2);
      bool grow = false;
      #pragma unroll
      for (int i = 0; i < NQ; ++i) {
        const float pb = vmax3(S[2][i][0], S[2][i][1], S[2][i][2]);
        pa[i] = vmax3(pa[i], S[2][i][3], S[3][i][0]); pa[i] = vmax3(pa[i], S[3][i][1], S[3][i][2]);
        pa[i] = vmax3(pa[i], pb, S[3][i][3]);
      }
      grow = (NQ == 2 ? __builtin_elementwise_maximum(pa[0], pa[NQ - 1]) : pa[0]) > THR;
      if (__builtin_expect(t == 0 || __any(grow), 0)) {
        #pragma unroll
        for (int i = 0; i < NQ; ++i) {
          const float pmx = xg_max(pa[i]);
          const float delta = (t == 0) ? pmx : fmaxf(pmx, 0.f);
          const float alpha = (t == 0) ? 0.f : EXP2(-delta);
          m[i] += delta; l[i] *= alpha;
          negm[i] = f32x4{-m[i], -m[i], -m[i], -m[i]};
          #pragma unroll
          for (int kt = 0; kt < 4; ++kt) S[kt][i] -= delta;
          #pragma unroll
          for (int d = 0; d < 8; ++d) O[d][i] *= alpha;
          #pragma unroll
          for (int k2 = 0; k2 < 2; ++k2)
            #pragma unroll
            for (int e = 0; e < 4; e += 2) pw[i][0][2 * k2 + (e >> 1)] = cvtpk(EXP2(S[k2][i][e]), EXP2(S[k2][i][e + 1]));
        }
        SB();
        NOP2();
      }
      if (KPOS == 1 && kdma) issue_kv(c, c.srdK, sK, kdst, vk0, vk1, vk2, vk3);
      waitlgkm<15>();
      SB();
      TICK(3);
      #pragma unroll
      for (int i = 0; i < NQ; ++i) MFMA_ACC(l[i], ones, __builtin_bit_cast(bf16x8, pw[i][0]));
      #pragma unroll
      for (int d = 0; d < 8; ++d) {
        #pragma unroll
        for (int i = 0; i < NQ; ++i) MFMA_ACC(O[d][i], vf[d], __builtin_bit_cast(bf16x8, pw[i][0]));
        if (d < 4) {
          const int k2 = 2 + (d >> 1), e = 2 * (d & 1);
          #pragma unroll
          for (int i = 0; i < NQ; ++i) { u32 pk = cvtpk(EXP2(S[k2][i][e]), EXP2(S[k2][i][e + 1])); PIN(pk); pw[i][1][2 * (k2 - 2) + (e >> 1)] = pk; }
        }
        SB();
      }
      u32 ka[4];
      ka[0] = kbase + kA;
      #pragma unroll
      for (int ks = 1; ks < 4; ++ks) ka[ks] = ka[0] ^ (64u * ks);
      if (KPOS == 2 && kdma) issue_kv(c, c.srdK, sK, kdst, vk0, vk1, vk2, vk3);
      waitlgkm<0>();
      SB();
      #pragma unroll
      for (int i = 0; i < NQ; ++i) MFMA_ACC(l[i], ones, __builtin_bit_cast(bf16x8, pw[i][1]));
      #pragma unroll
      for (int d = 0; d < 8; ++d) {
        #pragma unroll
        for (int i = 0; i < NQ; ++i) MFMA_ACC(O[d][i], vf[8 + d], __builtin_bit_cast(bf16x8, pw[i][1]));
        SB();
        kf[2 * d] = lds_b128(L, ka[(2 * d) & 3] + 4096u * ((2 * d) >> 2));
        kf[2 * d + 1] = lds_b128(L, ka[(2 * d + 1) & 3] + 4096u * ((2 * d + 1) >> 2));
        SB();
      }
      s3 = s3n;
      TICK(4);
      if (NQ == 1 && rd == 3 && t == 0) barrier_vm<5>();
      else if (kdma) barrier_vm<4>(); else barrier_vm<0>();
      TICK(5);
    }
    if (rd < 3) {
      #pragma unroll
      for (int i = 0; i < NQ; ++i)
        #pragma unroll
        for (int ks = 0; ks < 4; ++ks) qf[i][ks] = lds_b128(L, (qA ^ (64u * ks)) + 4096u * i);
    }
    const u32 orow = rowq - ((rd == 3) ? L_QH : 0u);
    NOP8();
    SB();
    #pragma unroll
    for (int i = 0; i < NQ; ++i) {
      const float inv = 1.0f / l[i][0];
      #pragma unroll
      for (int d = 0; d < 8; ++d) lds_w64(L, orow + 4096u * i + (oW ^ (32u * d)), pack4(O[d][i], inv));
    }
    waitlgkm<0>();
    if (NQ == 1 && rd == 2) {
      if (ln < 8) { *lp<u64>(L_FLAG + 8u * ln) = d0; st_agent((char*)p.ws + WS_ACK + 128u * c.bid + 8u * ln, d0 ^ ACK_MIX); }
    }
    STAMP(2 + rd);
  }
  TICKOUT
}

template <int NQ>
DI void projection_role(const Params& p, Ctx& c, bf16x8 (&park)[16]) {
  LAS char* L = c.L;
  const u32 w = c.w, wave = c.wave; (void)wave;
  const u32 b = c.b, j = c.j; (void)j;
  const int ln = lane_id();
  const u32 cl = ln & 15, g = ln >> 4, q4 = cl >> 2, p4 = ln & 3;
  const u32 vv0 = 256u * g + 16u * (cl ^ (2u * g)), vv2 = vv0 ^ 128u;
  const __amdgpu_buffer_rsrc_t srdW = mksrd(p.wq[c.br], DM * NH * DH * 2);
  const u32 rot = KROT, dh0 = DROT;
  const u32 vo_w = wq_pattern(c, ln);
  auto issue_w = [&](u32 rdn, u32 pi, u32 la) { issue_wq(c, srdW, vo_w, rdn, pi, la); };
  Proj<NQ> pj;
  pj.init(c, ln);
  const u32 rowq = pj.rowq;
  const u32 qA = rowq + 16u * (g ^ cl);
  STAMP(13);
  issue_w(0, 0, 7 * SLOT); issue_x_rows(p, c, ln); issue_w(0, 1, 9 * SLOT);
  STAMP(14);
  barrier_vm<4>();
  STAMP(1);
  pj.read_x(c, ln);
  barrier_lgkm();
  issue_w(0, 2, 5 * SLOT);
  pj.template piece<PFP>(c, 0, 7 * SLOT, dh0);
  barrier_vm<4>();
  STAMP(2);
  issue_w(0, 3, 6 * SLOT);
  pj.template piece<PFP>(c, 1, 9 * SLOT, dh0);
  barrier_vm<4>();
  issue_w(1, 0, 7 * SLOT);
  pj.template piece<PFP>(c, 2, 5 * SLOT, dh0);
  barrier_vm<4>();
  STAMP(3);
  issue_kv(c, c.srdV, 0, L_V, vv0, vv0, vv2, vv2);
  pj.template piece<PFP>(c, 3, 6 * SLOT, dh0);
  issue_kv(c, c.srdV, 1, L_V + SLOT, vv0, vv0, vv2, vv2);
  barrier_lgkm();
  STAMP(4);
  barrier_vm<4>();
  STAMP(5);
  bf16x8 og1[NQ][4];
  #pragma unroll
  for (int i = 0; i < NQ; ++i)
    #pragma unroll
    for (int ks = 0; ks < 4; ++ks) og1[i][ks] = bf16x8{};
  if (NQ == 1) {
    #pragma unroll
    for (int i = 4; i < 16; ++i) park[i] = bf16x8{};
  }
  u32 s3 = 0;
  TICKDECL
  #pragma unroll 1
  for (int hh = 0; hh < 6; ++hh) {
    #pragma unroll
    for (int kq = 0; kq < 4; ++kq) {
      const u32 s = 4u * hh + kq, s1 = s + 1;
      const u32 vdst = L_V + ((s3 == 0) ? 2 : s3 - 1) * SLOT;
      TICK(0);
      issue_w((1 + (s1 >> 3)) & 3, s1 & 7, L_W + (kq & 1) * SLOT);
      TICK(1);
      if (NQ == 1 && kq == 1 && hh == 2) {
        #pragma unroll
        for (int i = 4; i < 16; ++i) park[i] = lds_b128(L, ((L_QH + 256u * cl + 16u * (g ^ cl)) ^ (64u * (i & 3))) + 4096u * (2 + (i >> 2)));
      }
      if (kq == 1 && hh == 4) {
        #pragma unroll
        for (int i = 0; i < NQ; ++i)
          #pragma unroll
          for (int ks = 0; ks < 4; ++ks) og1[i][ks] = lds_b128(L, (qA ^ (64u * ks)) + 4096u * i);
      }
      pj.piece(c, kq, L_W + ((kq + 1) & 1) * SLOT, (u32)(hh & 1) ^ dh0);
      issue_kv(c, c.srdV, s + 2, vdst, vv0, vv0, vv2, vv2);
      s3 = (s3 == 2) ? 0 : s3 + 1;
      TICK(2);
      barrier_vm<4>();
      TICK(4);
    }
  }
  #pragma unroll
  for (int i = 0; i < 4 * NQ; ++i) park[i] = bf16x8{};
  const __amdgpu_buffer_rsrc_t srdO = mksrd(p.wo[c.br], NH * DH * DM * 2);
  #pragma unroll 1
  for (u32 s = 24; s < 32; ++s) {
    const u32 vdst = L_V + ((s3 == 0) ? 2 : s3 - 1) * SLOT;
    if (s + 2 < 32) issue_kv(c, c.srdV, s + 2, vdst, vv0, vv0, vv2, vv2);
    dma_w(rfl(c.ldsb + L_W + 1024u * w), 16u * ln, srdO, rfl((c.hg * 512u + 32u * b + 4u * (s - 24) + w) * 1024u));
    if (s == 25) {
      #pragma unroll
      for (int i = 0; i < NQ; ++i)
        #pragma unroll
        for (int ks = 0; ks < 4; ++ks) park[4 * i + ks] = lds_b128(L, (qA ^ (64u * ks)) + 4096u * i);
    }
    s3 = (s3 == 2) ? 0 : s3 + 1;
    if (s < 30) barrier_vm<5>(); else barrier_vm<0>();
  }
  STAMP(6);
  TICKOUT
  #pragma unroll
  for (int i = 0; i < NQ; ++i)
    #pragma unroll
    for (int ks = 0; ks < 4; ++ks) lds_w128(L, OREG + ((qA - L_QH) ^ (64u * ks)) + 4096u * i, og1[i][ks]);
}

__global__ __launch_bounds__(512, 2) void fused_attn(Params p) {
  extern __shared__ __attribute__((aligned(16))) char smem[];
  Ctx c;
  c.L = (LAS char*)smem;
  c.ldsb = (u32)(size_t)smem;
  c.wave = rfl(threadIdx.x >> 6);
  c.w = c.wave & 3;
  const u32 wave = c.wave, w = c.w;
  const u32 bid = blockIdx.x, xcd = bid & 7;
  c.j = bid >> 3; c.bid = bid;
  c.br = xcd & 1; c.hg = 2 * (xcd >> 1) + (c.j & 1); c.b = c.j >> 1;
  const size_t kvoff = (size_t)((c.b * NH + c.hg * 4) * SK) * DH * 2;
  c.srdK = mksrd((const char*)p.k[c.br] + kvoff, 4 * SK * DH * 2);
  c.srdV = mksrd((const char*)p.v[c.br] + kvoff, 4 * SK * DH * 2);
  STAMP(0); STAMPC(8);
  bf16x8 park[16];
  if (wave < 3) attention_role<2>(p, c, park);
  else if (wave == 3) attention_role<1>(p, c, park);
  else if (wave < 7) projection_role<2>(p, c, park);
  else projection_role<1>(p, c, park);

  {
    LAS char* L = c.L;
    const int ln = lane_id();
    const u32 cl = ln & 15, g = ln >> 4, q4 = cl >> 2, p4 = ln & 3;
    const __amdgpu_buffer_rsrc_t srdO = mksrd(p.wo[c.br], NH * DH * DM * 2);
    const u32 vo0 = 1024u * (ln >> 3) + 16u * ((ln & 7) ^ (2u * ((ln >> 4) & 1))), vo1 = 1024u * (ln >> 3) + 16u * ((ln & 7) ^ (2u * (((ln >> 4) & 1) + 2u)));
    const u32 ring = L_RING + wave * (3 * PIECE);
    auto issue_o = [&](u32 u, u32 slot3) {
      const u32 hs = (0x2013u >> (4u * ((u >> 2) & 3))) & 3u;
      const u32 so = (u < 16) ? ((c.hg * 4 + hs) * 128u + 32u * (u & 3)) * 1024u + 128u * wave : 0x01000000u;
      const u32 lb = c.ldsb + ring + slot3 * PIECE;
      dma_wo(rfl(lb), vo0, srdO, rfl(so));                     dma_wo(rfl(lb + 1024u), vo1, srdO, rfl(so + 8192u));
      dma_wo(rfl(lb + 2048u), vo0, srdO, rfl(so + 16384u));     dma_wo(rfl(lb + 3072u), vo1, srdO, rfl(so + 24576u));
    };
    const char* ackp = (const char*)p.ws + WS_ACK + 128u * member_bid(c, ln & 7) + 8u * c.hg;
    u64 ack;
    asm volatile("global_load_dwordx2 %0, %1, off sc1" : "=&v"(ack) : "v"(ackp) : "memory");
    issue_o(0, 0); issue_o(1, 1); issue_o(2, 2);
    barrier_lgkm();
    STAMP(7);
    const u32 oA = 256u * cl + 16u * (g ^ cl);
    const u32 wA = 128u * (8u * g + q4) + 16u * ((2u * ((q4 >> 1) + 2u * (g & 1))) | (p4 >> 1)) + 8u * (p4 & 1);
    auto aread = [&](bf16x8 (&af)[4], u32 slot3) {
      const u32 a0 = ring + slot3 * PIECE + wA;
      #pragma unroll
      for (int nt = 0; nt < 4; ++nt) { const u32 a = a0 ^ (32u * nt); af[nt] = lds_tr2(L, a, a + 512u); }
    };
    f32x4 acc[4][7];
    bf16x8 afA[4], afB[4], bq[7];
    auto oaddr = [&](u32 u) { return OREG * ((u >> 2) & 1) + (oA ^ (64u * (u & 3))); };
    SB();
    waitvm<8>();
    asm volatile("" : "+v"(ack));
    aread(afA, 0);
    {
      const u32 ob = oaddr(0);
      #pragma unroll
      for (int qt = 0; qt < 4; ++qt) bq[qt] = lds_b128(L, ob + 4096u * qt);
    }
    u32 sl = 0;
    auto kstep = [&](const u32 u, bf16x8 (&af)[4], bf16x8 (&afn)[4]) {
      const u32 sl1 = (sl == 2) ? 0 : sl + 1;
      const u32 ob = oaddr(u), obn = oaddr(u + 1);
      const bool pre = (u & 7) != 7;
      #pragma unroll
      for (int qt = 0; qt < 7; ++qt) {
        #pragma unroll
        for (int nt = 0; nt < 4; ++nt) { if (u == 0) MFMA_ZERO(acc[nt][qt], af[nt], bq[qt]); else MFMA_ACC(acc[nt][qt], af[nt], bq[qt]); }
        SB();
        if (qt < 3) bq[qt + 4] = lds_b128(L, ob + 4096u * (qt + 4));
        else if (pre) bq[qt - 3] = lds_b128(L, obn + 4096u * (qt - 3));
        if (qt == 0) issue_o(u + 3, sl);
        if (qt == 2) { waitvm<8>(); aread(afn, sl1); }
        SB();
      }
      sl = sl1;
    };
    #pragma unroll
    for (u32 u = 0; u < 16; u += 2) {
      if (u == 8) {
        barrier_lgkm();
        if (wave == 3 || wave == 7) {
          #pragma unroll
          for (int i = 0; i < 16; ++i) {
            if (wave == 3) lds_w128(L, (oA ^ (64u * (i & 3))) + 4096u * ((i >> 2) == 3 ? 6 : (i >> 2)), park[i]);
            else if (i >= 4) lds_w128(L, (oA ^ (64u * (i & 3))) + 4096u * (2 + (i >> 2)), park[i]);
          }
        }
        if (wave >= 4) {
          #pragma unroll
          for (int ks = 0; ks < 4; ++ks) {
            lds_w128(L, OREG + (oA ^ (64u * ks)) + 8192u * w, park[ks]);
            if (w < 3) lds_w128(L, OREG + (oA ^ (64u * ks)) + 8192u * w + 4096u, park[4 + ks]);
          }
        }
        barrier_lgkm();
        const u32 ob = oaddr(8);
        #pragma unroll
        for (int qt = 0; qt < 4; ++qt) bq[qt] = lds_b128(L, ob + 4096u * qt);
      }
      kstep(u, afA, afB);
      kstep(u + 1, afB, afA);
    }
    waitvm<0>();
    STAMP(11);
    NOP8();
    SB();
    const __amdgpu_buffer_rsrc_t srdP = mksrd((const char*)p.ws + WS_P + (size_t)(c.br * NB + c.b) * 8 * PART_WG, 8 * PART_WG);
    #pragma unroll
    for (int qt = 0; qt < 7; ++qt)
      #pragma unroll
      for (int np = 0; np < 2; ++np) {
        const u32x2 a = pack4n(acc[2 * np][qt]), bb = pack4n(acc[2 * np + 1][qt]);
        __builtin_amdgcn_raw_buffer_store_b128(u32x4{a[0], a[1], bb[0], bb[1]}, srdP, c.hg * PART_WG + wave * 14336u + (qt * 2 + np) * 1024u + ln * 16u, 0, 16);
      }
    waitvm<0>();
    barrier_lgkm();
    STAMP(11);
    if (wave == 0) {
      const u64 e0 = *lp<u64>(L_FLAG + 8u * (ln & 7));
      const u64 want = *lp<u64>(L_FLAG + 8u * c.hg) ^ ACK_MIX;
      for (u32 spin = 0; !__all(ack == want) && spin < SPIN_CAP; ++spin) { __builtin_amdgcn_s_sleep(8); ack = ld_agent(ackp); }
      if (ln == (int)c.hg) st_agent((char*)p.ws + WS_DONE + 128u * c.bid, e0 + 1);
      const char* dp = (const char*)p.ws + WS_DONE + 128u * member_bid(c, ln & 7);
      for (u32 spin = 0; spin < SPIN_CAP; ++spin) { if (__all(ld_agent(dp) == e0 + 1)) break; __builtin_amdgcn_s_sleep(2); }
    }
    barrier_lgkm();
    STAMP(12);
    if (wave < 7) {
      u32x4 x[2][8];
      #pragma unroll
      for (int np = 0; np < 2; ++np)
        #pragma unroll
        for (int m = 0; m < 8; ++m) x[np][m] = __builtin_amdgcn_raw_buffer_load_b128(srdP, m * PART_WG + c.hg * 14336u + (wave * 2 + np) * 1024u + ln * 16u, 0, 16);
      const u32 tile = wave * 2048u;
      #pragma unroll
      for (int np = 0; np < 2; ++np) {
        float s8[8] = {};
        #pragma unroll
        for (int m = 0; m < 8; ++m)
          #pragma unroll
          for (int i = 0; i < 4; ++i) { s8[2 * i] += __uint_as_float(x[np][m][i] << 16); s8[2 * i + 1] += __uint_as_float(x[np][m][i] & 0xffff0000u); }
        lds_w64(L, tile + cl * 128u + 64u * np + 8u * g, u32x2{cvtpk(s8[0], s8[1]), cvtpk(s8[2], s8[3])});
        lds_w64(L, tile + cl * 128u + 64u * np + 32u + 8u * g, u32x2{cvtpk(s8[4], s8[5]), cvtpk(s8[6], s8[7])});
      }
      #pragma unroll
      for (int i = 0; i < 2; ++i) {
        const u32 row = 8u * i + (ln >> 3), ch = ln & 7;
        char* o = (char*)p.out + (((size_t)c.b * SQ + 16u * wave + row) * (2 * DM) + c.br * DM + 64u * c.hg) * 2 + 16u * ch;
        *(bf16x8*)o = lds_b128(L, tile + row * 128u + 16u * ch);
      }
    }
  }
  STAMP(10); STAMPC(9);
  STAMPOUT();
}

extern "C" void kernel_launch(void* const* d_in, const int* in_sizes, int n_in, void* d_out, int out_size, void* d_ws, size_t ws_size,
                              hipStream_t stream) {
  Params p;
  for (int i = 0; i < 2; ++i) { p.x[i] = d_in[5 * i]; p.k[i] = d_in[5 * i + 1]; p.v[i] = d_in[5 * i + 2]; p.wq[i] = d_in[5 * i + 3]; p.wo[i] = d_in[5 * i + 4]; }
  p.out = d_out; p.ws = d_ws;
  (void)hipFuncSetAttribute((const void*)fused_attn, hipFuncAttributeMaxDynamicSharedMemorySize, LDS_BYTES);
  fused_attn<<<256, 512, LDS_BYTES, stream>>>(p);
}
```

```cpp
#include <hip/hip_runtime.h>
#include <hip/hip_bf16.h>

typedef unsigned u32;
typedef unsigned long long u64;
typedef __bf16 bf16x8 __attribute__((ext_vector_type(8)));
typedef __bf16 bf16x4 __attribute__((ext_vector_type(4)));
typedef __bf16 bf16x2 __attribute__((ext_vector_type(2)));
typedef float f32x4 __attribute__((ext_vector_type(4)));
typedef float f32x2 __attribute__((ext_vector_type(2)));
typedef u32 u32x4 __attribute__((ext_vector_type(4)));
typedef u32 u32x2 __attribute__((ext_vector_type(2)));
#define LAS __attribute__((address_space(3)))
#define DI __device__ __forceinline__

constexpr int NB = 16, SQ = 112, DM = 512, NH = 32, SK = 512, DH = 128;
constexpr float QSCALE = 0.12751743082f;
constexpr float THR = 8.0f;

constexpr u32 SLOT = 16384;
constexpr u32 L_K = 0;
constexpr u32 L_V = 3 * SLOT;
constexpr u32 L_W = 6 * SLOT;
constexpr u32 L_QH = 8 * SLOT;
constexpr u32 LDS_BYTES = 10 * SLOT;
constexpr u32 OREG = 28672;
constexpr u32 L_RING = 2 * OREG;
constexpr u32 PIECE = 4096;

constexpr size_t WS_P = 0;
constexpr u32 PART_WG = 8 * 7 * 2 * 1024;
constexpr size_t WS_DONE = 32u << 20, WS_ACK = WS_DONE + 65536;
constexpr u32 L_FLAG = 159744;
constexpr unsigned long long ACK_MIX = 0x9E3779B97F4A7C15ull;
constexpr u32 SPIN_CAP = 1u << 18;

struct Params {
  const void* x[2]; const void* k[2]; const void* v[2]; const void* wq[2]; const void* wo[2];
  void* out; void* ws;
};

DI u32 rfl(u32 x) { return __builtin_amdgcn_readfirstlane(x); }
DI __amdgpu_buffer_rsrc_t mksrd(const void* p, u32 bytes) { return __builtin_amdgcn_make_buffer_rsrc((void*)p, (short)0, (int)bytes, 0x00020000); }
DI void dma_nt(u32 m0v, u32 voff, __amdgpu_buffer_rsrc_t srd, u32 soff) {
  asm volatile("s_mov_b32 m0, %0\n\ts_nop 0\n\tbuffer_load_dwordx4 %1, %2, %3 offen nt lds" :: "s"(m0v), "v"(voff), "s"(srd), "s"(soff) : "memory");
}
#define WPOL0 ""
#define WPOL1 ""
DI void dma_wo(u32 m0v, u32 voff, __amdgpu_buffer_rsrc_t srd, u32 soff) {
  asm volatile("s_mov_b32 m0, %0\n\ts_nop 0\n\tbuffer_load_dwordx4 %1, %2, %3 offen" WPOL1 " lds" :: "s"(m0v), "v"(voff), "s"(srd), "s"(soff) : "memory");
}
DI void dma_w(u32 m0v, u32 voff, __amdgpu_buffer_rsrc_t srd, u32 soff) {
  asm volatile("s_mov_b32 m0, %0\n\ts_nop 0\n\tbuffer_load_dwordx4 %1, %2, %3 offen" WPOL0 " lds" :: "s"(m0v), "v"(voff), "s"(srd), "s"(soff) : "memory");
}
#define KROTM 1
#define PF 3
#define PFP 6
#define WARMS 29
#define KPOS 1
#define KROT (KROTM == 1 ? (((b >> 2) + 2 * (j & 1)) & 3) : 0)
#define DROT (KROTM == 1 ? ((b >> 1) & 1) : 0)

template <int N> DI void waitvm() { asm volatile("s_waitcnt vmcnt(%0)" :: "i"(N) : "memory"); }
template <int N> DI void waitlgkm() { __builtin_amdgcn_s_waitcnt(0xC07F | (N << 8)); }
DI void barrier_lgkm() { asm volatile("" ::: "memory"); waitlgkm<0>(); __builtin_amdgcn_s_barrier(); asm volatile("" ::: "memory"); }
template <int N> DI void barrier_vm() { asm volatile("s_waitcnt vmcnt(%0)" :: "i"(N) : "memory"); waitlgkm<0>(); __builtin_amdgcn_s_barrier(); asm volatile("" ::: "memory"); }
DI u32 cvtpk(float lo, float hi) { f32x2 v = {lo, hi}; bf16x2 b = __builtin_convertvector(v, bf16x2); return __builtin_bit_cast(u32, b); }
DI u32x2 pack4(const f32x4& a, float s) { return u32x2{cvtpk(a[0] * s, a[1] * s), cvtpk(a[2] * s, a[3] * s)}; }
DI u32x2 pack4n(const f32x4& a) { return u32x2{cvtpk(a[0], a[1]), cvtpk(a[2], a[3])}; }
template <class T> DI LAS T* lp(u32 off) { return (LAS T*)off; }
DI bf16x8 lds_b128(LAS char*, u32 off) { return *lp<bf16x8>(off); }
DI void lds_w128(LAS char*, u32 off, bf16x8 v) { *lp<bf16x8>(off) = v; }
DI void lds_w64(LAS char*, u32 off, u32x2 v) { *lp<u32x2>(off) = v; }
DI bf16x8 lds_tr2(LAS char*, u32 off0, u32 off1) {
  bf16x4 a = __builtin_amdgcn_ds_read_tr16_b64_v4bf16(lp<bf16x4>(off0));
  bf16x4 b = __builtin_amdgcn_ds_read_tr16_b64_v4bf16(lp<bf16x4>(off1));
  return __builtin_shufflevector(a, b, 0, 1, 2, 3, 4, 5, 6, 7);
}
#define STAMP(i)
#define STAMPC(i)
#define STAMPOUT()
#define SB() __builtin_amdgcn_sched_barrier(0)
#define TICK(i)
#define TICKDECL
#define TICKOUT
#define PIN(x) asm volatile("" : "+v"(x))
#define OPAQUE_S(x) asm volatile("" : "+s"(x))
DI float vmax3(float a, float b, float c) { return __builtin_elementwise_maximum(__builtin_elementwise_maximum(a, b), c); }
#define MFMA_ACC(acc, a, b) asm volatile("v_mfma_f32_16x16x32_bf16 %0, %1, %2, %0" : "+v"(acc) : "v"(a), "v"(b))
#define MFMA_INIT(acc, a, b, c) asm volatile("v_mfma_f32_16x16x32_bf16 %0, %1, %2, %3" : "=&v"(acc) : "v"(a), "v"(b), "v"(c))
#define MFMA_ZERO(acc, a, b) asm volatile("v_mfma_f32_16x16x32_bf16 %0, %1, %2, 0" : "=&v"(acc) : "v"(a), "v"(b))
#define NOP8() asm volatile("s_nop 7")
#define NOP2() asm volatile("s_nop 1")
#define EXP2(x) __builtin_amdgcn_exp2f(x)
#define GAS __attribute__((address_space(1)))
DI u64 ld_agent(const char* p) { return __hip_atomic_load((const GAS u64*)p, __ATOMIC_RELAXED, __HIP_MEMORY_SCOPE_AGENT); }
DI void st_agent(char* p, u64 v) { __hip_atomic_store((GAS u64*)p, v, __ATOMIC_RELAXED, __HIP_MEMORY_SCOPE_AGENT); }
DI int lane_id() { int ln; asm volatile("v_mbcnt_lo_u32_b32 %0, -1, 0\n\tv_mbcnt_hi_u32_b32 %0, -1, %0" : "=v"(ln)); return ln; }
DI float xg_max(float x) {
  auto a = __builtin_amdgcn_permlane16_swap(__float_as_uint(x), __float_as_uint(x), false, false);
  x = fmaxf(__uint_as_float(a[0]), __uint_as_float(a[1]));
  auto b = __builtin_amdgcn_permlane32_swap(__float_as_uint(x), __float_as_uint(x), false, false);
  return fmaxf(__uint_as_float(b[0]), __uint_as_float(b[1]));
}
DI float xg_sum(float x) {
  auto a = __builtin_amdgcn_permlane16_swap(__float_as_uint(x), __float_as_uint(x), false, false);
  x = __uint_as_float(a[0]) + __uint_as_float(a[1]);
  auto b = __builtin_amdgcn_permlane32_swap(__float_as_uint(x), __float_as_uint(x), false, false);
  return __uint_as_float(b[0]) + __uint_as_float(b[1]);
}

struct Ctx {
  LAS char* L; u32 ldsb, wave, w, br, b, hg, j, bid;
  __amdgpu_buffer_rsrc_t srdK, srdV;
};

DI u32 member_bid(const Ctx& c, u32 m) { return 8u * (2u * c.b + (m & 1u)) + c.br + 2u * (m >> 1); }

DI void issue_kv(Ctx& c, const __amdgpu_buffer_rsrc_t srd, u32 st, u32 la, u32 vo0, u32 vo1, u32 vo2, u32 vo3) {
  asm volatile("s_mov_b32 m0, %0\n\ts_nop 0\n\t"
               "buffer_load_dwordx4 %1, %5, %6 offen nt lds\n\t"
               "buffer_load_dwordx4 %2, %5, %6 offen offset:1024 nt lds\n\t"
               "buffer_load_dwordx4 %3, %5, %6 offen offset:2048 nt lds\n\t"
               "buffer_load_dwordx4 %4, %5, %6 offen offset:3072 nt lds"
               :: "s"(rfl(c.ldsb + la + 4096u * c.w)), "v"(vo0), "v"(vo1), "v"(vo2), "v"(vo3), "s"(srd), "s"(rfl(st * SLOT + 4096u * c.w)) : "memory");
}

DI u32 wq_pattern(const Ctx& c, int ln) { return 8192u * (ln >> 3) + 16u * ((ln & 7) ^ (2u * (((ln >> 4) & 1) + 2u * (c.w & 1)))); }
DI void issue_wq(const Ctx& c, const __amdgpu_buffer_rsrc_t srdW, u32 vo_w, u32 rdn, u32 pi, u32 la) {
  const u32 b = c.b, j = c.j; (void)j; (void)b;
  const u32 hd = c.hg * 4 + rdn;
  const u32 kq = ((pi & 3) + KROT) & 3, dh = (pi >> 2) ^ DROT;
  const u32 so = (128u * kq + 8u * c.w) * 8192u + (128u * hd + 64u * dh) * 2u;
  #pragma unroll
  for (int i = 0; i < 4; ++i) dma_w(rfl(c.ldsb + la + 1024u * c.w + 4096u * i), vo_w, srdW, rfl(so + 32u * 8192u * i));
}
DI void issue_x_rows(const Params& p, Ctx& c, int ln) {
  const __amdgpu_buffer_rsrc_t srdX = mksrd((const char*)p.x[c.br] + (size_t)c.b * SQ * DM * 2, SQ * DM * 2);
  const u32 start = 56u * (c.j & 1);
  auto one = [&](u32 row) {
    row += start; row = row >= 112u ? row - 112u : row;
    dma_w(rfl(c.ldsb + 1024u * row), (16u * ln) ^ rfl(16u * (row & 15u)), srdX, rfl(1024u * row));
  };
  if (c.wave < 4) {
    #pragma unroll
    for (int i = 0; i < 16; ++i) one(c.w + 4u * i);
  } else {
    #pragma unroll
    for (int i = 0; i < 12; ++i) one(64u + c.w + 4u * i);
  }
}

template <int NQ> struct Proj {
  bf16x8 xf[NQ][16];
  f32x4 qa[4][NQ];
  u32 wA, rowq, oW;
  DI void init(const Ctx& c, int ln) {
    const u32 cl = ln & 15, g = ln >> 4, q4 = cl >> 2, p4 = ln & 3;
    wA = 128u * (8u * g + q4) + 16u * ((2u * ((q4 >> 1) + 2u * (g & 1))) | (p4 >> 1)) + 8u * (p4 & 1);
    rowq = L_QH + 256u * (32u * c.w + cl);
    oW = 16u * ((g >> 1) ^ cl) + 8u * (g & 1);
  }
  DI void read_x(const Ctx& c, int ln) {
    const u32 b = c.b, j = c.j; (void)j; (void)b;
    const u32 cl = ln & 15, g = ln >> 4;
    const u32 xA = 1024u * (32u * c.w + cl) + 16u * (g ^ cl);
    #pragma unroll
    for (int jq = 0; jq < 4; ++jq) {
      const u32 a = xA + 256u * (((u32)jq + KROT) & 3u);
      #pragma unroll
      for (int i = 0; i < NQ; ++i)
        #pragma unroll
        for (int ks = 0; ks < 4; ++ks) xf[i][4 * jq + ks] = lds_b128(c.L, (a ^ (64u * ks)) + 16384u * i);
    }
  }
  template <int PFV = PF>
  DI void piece(const Ctx& c, const int kq, u32 la, u32 dh) {
    LAS char* L = c.L;
    OPAQUE_S(la);
    u32 wa[4];
    #pragma unroll
    for (int d = 0; d < 4; ++d) wa[d] = la + (wA ^ (32u * d));
    auto wread = [&](const int n) { return lds_tr2(L, wa[n & 3] + 4096u * (n >> 2), wa[n & 3] + 4096u * (n >> 2) + 512u); };
    bf16x8 fr[16];
    #pragma unroll
    for (int n = 0; n < 2 * PFV; ++n) fr[n] = wread(n);
    SB();
    #pragma unroll
    for (int hp = 0; hp < 8; ++hp) {
      const int young = 4 * ((7 - hp) < (PFV - 1) ? (7 - hp) : (PFV - 1));
      if (young >= 12) waitlgkm<12>(); else if (young == 8) waitlgkm<8>(); else if (young == 4) waitlgkm<4>(); else waitlgkm<0>();
      SB();
      #pragma unroll
      for (int e = 0; e < 2; ++e) {
        const int n = 2 * hp + e, ks = n >> 2, d = n & 3;
        #pragma unroll
        for (int i = 0; i < NQ; ++i) { if (kq == 0 && ks == 0) MFMA_ZERO(qa[d][i], fr[n], xf[i][4 * kq + ks]); else MFMA_ACC(qa[d][i], fr[n], xf[i][4 * kq + ks]); }
      }
      SB();
      if (hp + PFV < 8) { fr[2 * (hp + PFV)] = wread(2 * (hp + PFV)); fr[2 * (hp + PFV) + 1] = wread(2 * (hp + PFV) + 1); }
      SB();
    }
    if (kq == 3) {
      NOP8();
      SB();
      #pragma unroll
      for (int i = 0; i < NQ; ++i)
        #pragma unroll
        for (int d = 0; d < 4; ++d) lds_w64(L, rowq + 4096u * i + (oW ^ (32u * (4u * dh + d))), pack4(qa[d][i], QSCALE));
    }
  }
};

template <int NQ>
DI void attention_role(const Params& p, Ctx& c, bf16x8 (&park)[16]) {
  LAS char* L = c.L;
  const u32 w = c.w, wave = c.wave; (void)wave;
  const int ln = lane_id();
  const u32 cl = ln & 15, g = ln >> 4, q4 = cl >> 2, p4 = ln & 3;
  const u32 pi_ = (cl & 3) | ((cl & 4) << 1) | ((cl & 8) >> 1);
  const u32 vk0 = 256u * g + 16u * (cl ^ g), vk1 = vk0 ^ 64u, vk2 = vk0 ^ 128u, vk3 = vk0 ^ 192u;
  const u32 kA = 256u * pi_ + 16u * (g ^ pi_);
  const u32 vA = 256u * (8u * (g & 1) + 4u * (g >> 1) + q4) + 16u * ((2u * (q4 + 4u * (g & 1))) | (p4 >> 1)) + 8u * (p4 & 1);
  const u32 rowq = L_QH + 256u * (32u * w + cl);
  const u32 qA = rowq + 16u * (g ^ cl);
  const u32 oW = 16u * ((g >> 1) ^ cl) + 8u * (g & 1);
  u64 d0 = 0;
  {
    const u32 b = c.b, j = c.j; (void)j; (void)b;
    const __amdgpu_buffer_rsrc_t srdW = mksrd(p.wq[c.br], DM * NH * DH * 2);
    const u32 vo_w = wq_pattern(c, ln);
    const u32 dh1 = DROT ^ 1u;
    Proj<NQ> pj;
    pj.init(c, ln);
    if (NQ == 1) d0 = ld_agent((const char*)p.ws + WS_DONE + 128u * member_bid(c, ln & 7));
    issue_x_rows(p, c, ln); issue_wq(c, srdW, vo_w, 0, 4, 8 * SLOT);
    barrier_vm<4>();
    if (NQ == 1) PIN(d0);
    pj.read_x(c, ln);
    barrier_vm<0>();
    issue_wq(c, srdW, vo_w, 0, 5, 3 * SLOT); issue_wq(c, srdW, vo_w, 0, 6, 4 * SLOT);
    pj.template piece<PFP>(c, 0, 8 * SLOT, dh1);
    barrier_vm<4>();
    issue_wq(c, srdW, vo_w, 0, 7, 2 * SLOT);
    pj.template piece<PFP>(c, 1, 3 * SLOT, dh1);
    barrier_vm<4>();
    issue_kv(c, c.srdK, 0, L_K, vk0, vk1, vk2, vk3);
    pj.template piece<PFP>(c, 2, 4 * SLOT, dh1);
    barrier_vm<4>();
    issue_kv(c, c.srdK, 1, L_K + SLOT, vk0, vk1, vk2, vk3);
    pj.template piece<PFP>(c, 3, 2 * SLOT, dh1);
    barrier_vm<4>();
  }
  issue_kv(c, c.srdK, 2, L_K + 2 * SLOT, vk0, vk1, vk2, vk3);
  bf16x8 kf[16];
  #pragma unroll
  for (int i = 0; i < 16; ++i) kf[i] = lds_b128(L, L_K + (kA ^ (64u * (i & 3))) + 4096u * (i >> 2));
  barrier_vm<4>();
  STAMP(1);
  bf16x8 qf[NQ][4];
  #pragma unroll
  for (int i = 0; i < NQ; ++i)
    #pragma unroll
    for (int ks = 0; ks < 4; ++ks) qf[i][ks] = lds_b128(L, (qA ^ (64u * ks)) + 4096u * i);
  waitlgkm<0>();
  if (NQ == 1) {
    #pragma unroll
    for (int i = 0; i < 16; ++i) park[i] = bf16x8{};
  }
  bf16x8 ones = __builtin_bit_cast(bf16x8, u32x4{0x3F803F80u, 0x3F803F80u, 0x3F803F80u, 0x3F803F80u});
  PIN(ones);
  u32 s3 = 0;
  f32x4 O[8][NQ], negm[NQ], l[NQ];
  float m[NQ];
  #pragma unroll
  for (int i = 0; i < NQ; ++i) {
    l[i] = f32x4{};
    #pragma unroll
    for (int d = 0; d < 8; ++d) O[d][i] = f32x4{};
  }
  TICKDECL
  #pragma unroll 1
  for (int rd = 0; rd < 4; ++rd) {
    #pragma unroll
    for (int i = 0; i < NQ; ++i) { m[i] = 0.f; negm[i] = f32x4{}; }
    #pragma unroll 1
    for (int t = 0; t < 8; ++t) {
      const u32 s3n = (s3 == 2) ? 0 : s3 + 1;
      const u32 vbase = L_V + s3 * SLOT, kbase = L_K + s3n * SLOT;
      const u32 sK = 8u * rd + t + 3;
      const bool kdma = sK < 32;
      TICK(0);
      const u32 kdst = L_K + s3 * SLOT;
      if (KPOS == 0 && kdma) issue_kv(c, c.srdK, sK, kdst, vk0, vk1, vk2, vk3);
      TICK(1);
      if (NQ == 1 && rd == 1 && t == 1) {
        #pragma unroll
        for (int i = 0; i < 16; ++i) park[i] = lds_b128(L, ((L_QH + 256u * cl + 16u * (g ^ cl)) ^ (64u * (i & 3))) + 4096u * ((i >> 2) == 3 ? 6 : (i >> 2)));
      }
      u32 va[8];
      va[0] = vbase + vA;
      #pragma unroll
      for (int d = 1; d < 8; ++d) va[d] = va[0] ^ (32u * d);
      auto vread = [&](const int d, const int u) { return lds_tr2(L, va[d] + 8192u * u, va[d] + 8192u * u + 4096u); };
      f32x4 S[4][NQ];
      bf16x8 vf[16];
      #pragma unroll
      for (int kt = 0; kt < 2; ++kt)
        #pragma unroll
        for (int ks = 0; ks < 4; ++ks) {
          #pragma unroll
          for (int i = 0; i < NQ; ++i) { if (ks == 0) MFMA_INIT(S[kt][i], kf[4 * kt + ks], qf[i][ks], negm[i]); else MFMA_ACC(S[kt][i], kf[4 * kt + ks], qf[i][ks]); }
          vf[4 * kt + ks] = vread(4 * kt + ks, 0);
          SB();
        }
      u32x4 pw[NQ][2];
      float pa[NQ];
      #pragma unroll
      for (int kt = 2; kt < 4; ++kt)
        #pragma unroll
        for (int ks = 0; ks < 4; ++ks) {
          const int n = 4 * (kt - 2) + ks;
          #pragma unroll
          for (int i = 0; i < NQ; ++i) { if (ks == 0) MFMA_INIT(S[kt][i], kf[4 * kt + ks], qf[i][ks], negm[i]); else MFMA_ACC(S[kt][i], kf[4 * kt + ks], qf[i][ks]); }
          vf[8 + n] = vread(n, 1);
          if (n >= 2 && n < 6) {
            const int k2 = (n - 2) >> 1, e = 2 * ((n - 2) & 1);
            #pragma unroll
            for (int i = 0; i < NQ; ++i) {
              const float x0 = S[k2][i][e], x1 = S[k2][i][e + 1];
              pa[i] = (n == 2) ? __builtin_elementwise_maximum(x0, x1) : vmax3(pa[i], x0, x1);
              u32 pk = cvtpk(EXP2(x0), EXP2(x1)); PIN(pk);
              pw[i][0][2 * k2 + (e >> 1)] = pk;
            }
          }
          SB();
        }
      asm volatile("" ::: "memory");
      NOP8();
      SB();
      TICK(2);
      bool grow = false;
      #pragma unroll
      for (int i = 0; i < NQ; ++i) {
        const float pb = vmax3(S[2][i][0], S[2][i][1], S[2][i][2]);
        pa[i] = vmax3(pa[i], S[2][i][3], S[3][i][0]); pa[i] = vmax3(pa[i], S[3][i][1], S[3][i][2]);
        pa[i] = vmax3(pa[i], pb, S[3][i][3]);
      }
      grow = (NQ == 2 ? __builtin_elementwise_maximum(pa[0], pa[NQ - 1]) : pa[0]) > THR;
      if (__builtin_expect(t == 0 || __any(grow), 0)) {
        #pragma unroll
        for (int i = 0; i < NQ; ++i) {
          const float pmx = xg_max(pa[i]);
          const float delta = (t == 0) ? pmx : fmaxf(pmx, 0.f);
          m[i] += delta;
          negm[i] = f32x4{-m[i], -m[i], -m[i], -m[i]};
          #pragma unroll
          for (int kt = 0; kt < 4; ++kt) S[kt][i] -= delta;
          #pragma unroll
          for (int k2 = 0; k2 < 2; ++k2)
            #pragma unroll
            for (int e = 0; e < 4; e += 2) pw[i][0][2 * k2 + (e >> 1)] = cvtpk(EXP2(S[k2][i][e]), EXP2(S[k2][i][e + 1]));
          if (t == 0) {
            l[i] = f32x4{};
            #pragma unroll
            for (int d = 0; d < 8; ++d) O[d][i] = f32x4{};
          } else {
            const float alpha = EXP2(-delta);
            l[i] *= alpha;
            #pragma unroll
            for (int d = 0; d < 8; ++d) O[d][i] *= alpha;
          }
        }
        SB();
        NOP2();
      }
      if (KPOS == 1 && kdma) issue_kv(c, c.srdK, sK, kdst, vk0, vk1, vk2, vk3);
      waitlgkm<15>();
      SB();
      TICK(3);
      #pragma unroll
      for (int i = 0; i < NQ; ++i) MFMA_ACC(l[i], ones, __builtin_bit_cast(bf16x8, pw[i][0]));
      #pragma unroll
      for (int d = 0; d < 8; ++d) {
        #pragma unroll
        for (int i = 0; i < NQ; ++i) MFMA_ACC(O[d][i], vf[d], __builtin_bit_cast(bf16x8, pw[i][0]));
        if (d < 4) {
          const int k2 = 2 + (d >> 1), e = 2 * (d & 1);
          #pragma unroll
          for (int i = 0; i < NQ; ++i) { u32 pk = cvtpk(EXP2(S[k2][i][e]), EXP2(S[k2][i][e + 1])); PIN(pk); pw[i][1][2 * (k2 - 2) + (e >> 1)] = pk; }
        }
        SB();
      }
      u32 ka[4];
      ka[0] = kbase + kA;
      #pragma unroll
      for (int ks = 1; ks < 4; ++ks) ka[ks] = ka[0] ^ (64u * ks);
      if (KPOS == 2 && kdma) issue_kv(c, c.srdK, sK, kdst, vk0, vk1, vk2, vk3);
      waitlgkm<0>();
      SB();
      #pragma unroll
      for (int i = 0; i < NQ; ++i) MFMA_ACC(l[i], ones, __builtin_bit_cast(bf16x8, pw[i][1]));
      #pragma unroll
      for (int d = 0; d < 8; ++d) {
        #pragma unroll
        for (int i = 0; i < NQ; ++i) MFMA_ACC(O[d][i], vf[8 + d], __builtin_bit_cast(bf16x8, pw[i][1]));
        SB();
        kf[2 * d] = lds_b128(L, ka[(2 * d) & 3] + 4096u * ((2 * d) >> 2));
        kf[2 * d + 1] = lds_b128(L, ka[(2 * d + 1) & 3] + 4096u * ((2 * d + 1) >> 2));
        SB();
      }
      s3 = s3n;
      TICK(4);
      if (NQ == 1 && rd == 3 && t == 0) barrier_vm<5>();
      else if (kdma) barrier_vm<4>(); else barrier_vm<0>();
      TICK(5);
    }
    if (rd < 3) {
      #pragma unroll
      for (int i = 0; i < NQ; ++i)
        #pragma unroll
        for (int ks = 0; ks < 4; ++ks) qf[i][ks] = lds_b128(L, (qA ^ (64u * ks)) + 4096u * i);
    }
    const u32 orow = rowq - ((rd == 3) ? L_QH : 0u);
    NOP8();
    SB();
    u32x2 opk[NQ][8];
    #pragma unroll
    for (int i = 0; i < NQ; ++i) {
      const float inv = __builtin_amdgcn_rcpf(l[i][0]);
      #pragma unroll
      for (int d = 0; d < 8; ++d) opk[i][d] = pack4(O[d][i], inv);
    }
    waitlgkm<0>();
    SB();
    #pragma unroll
    for (int i = 0; i < NQ; ++i)
      #pragma unroll
      for (int d = 0; d < 8; ++d) lds_w64(L, orow + 4096u * i + (oW ^ (32u * d)), opk[i][d]);
    if (NQ == 1 && rd == 2) {
      if (ln < 8) { *lp<u64>(L_FLAG + 8u * ln) = d0; st_agent((char*)p.ws + WS_ACK + 128u * c.bid + 8u * ln, d0 ^ ACK_MIX); }
    }
    STAMP(2 + rd);
  }
  TICKOUT
}

template <int NQ>
DI void projection_role(const Params& p, Ctx& c, bf16x8 (&park)[16]) {
  LAS char* L = c.L;
  const u32 w = c.w, wave = c.wave; (void)wave;
  const u32 b = c.b, j = c.j; (void)j;
  const int ln = lane_id();
  const u32 cl = ln & 15, g = ln >> 4, q4 = cl >> 2, p4 = ln & 3;
  const u32 vv0 = 256u * g + 16u * (cl ^ (2u * g)), vv2 = vv0 ^ 128u;
  const __amdgpu_buffer_rsrc_t srdW = mksrd(p.wq[c.br], DM * NH * DH * 2);
  const u32 rot = KROT, dh0 = DROT;
  const u32 vo_w = wq_pattern(c, ln);
  auto issue_w = [&](u32 rdn, u32 pi, u32 la) { issue_wq(c, srdW, vo_w, rdn, pi, la); };
  Proj<NQ> pj;
  pj.init(c, ln);
  const u32 rowq = pj.rowq;
  const u32 qA = rowq + 16u * (g ^ cl);
  STAMP(13);
  issue_w(0, 0, 7 * SLOT); issue_x_rows(p, c, ln); issue_w(0, 1, 9 * SLOT);
  STAMP(14);
  barrier_vm<4>();
  STAMP(1);
  pj.read_x(c, ln);
  barrier_lgkm();
  issue_w(0, 2, 5 * SLOT);
  pj.template piece<PFP>(c, 0, 7 * SLOT, dh0);
  barrier_vm<4>();
  STAMP(2);
  issue_w(0, 3, 6 * SLOT);
  pj.template piece<PFP>(c, 1, 9 * SLOT, dh0);
  barrier_vm<4>();
  issue_w(1, 0, 7 * SLOT);
  pj.template piece<PFP>(c, 2, 5 * SLOT, dh0);
  barrier_vm<4>();
  STAMP(3);
  issue_kv(c, c.srdV, 0, L_V, vv0, vv0, vv2, vv2);
  pj.template piece<PFP>(c, 3, 6 * SLOT, dh0);
  issue_kv(c, c.srdV, 1, L_V + SLOT, vv0, vv0, vv2, vv2);
  barrier_lgkm();
  STAMP(4);
  barrier_vm<4>();
  STAMP(5);
  bf16x8 og1[NQ][4];
  #pragma unroll
  for (int i = 0; i < NQ; ++i)
    #pragma unroll
    for (int ks = 0; ks < 4; ++ks) og1[i][ks] = bf16x8{};
  if (NQ == 1) {
    #pragma unroll
    for (int i = 4; i < 16; ++i) park[i] = bf16x8{};
  }
  u32 s3 = 0;
  TICKDECL
  #pragma unroll 1
  for (int hh = 0; hh < 6; ++hh) {
    #pragma unroll
    for (int kq = 0; kq < 4; ++kq) {
      const u32 s = 4u * hh + kq, s1 = s + 1;
      const u32 vdst = L_V + ((s3 == 0) ? 2 : s3 - 1) * SLOT;
      TICK(0);
      issue_w((1 + (s1 >> 3)) & 3, s1 & 7, L_W + (kq & 1) * SLOT);
      TICK(1);
      if (NQ == 1 && kq == 1 && hh == 2) {
        #pragma unroll
        for (int i = 4; i < 16; ++i) park[i] = lds_b128(L, ((L_QH + 256u * cl + 16u * (g ^ cl)) ^ (64u * (i & 3))) + 4096u * (2 + (i >> 2)));
      }
      if (kq == 1 && hh == 4) {
        #pragma unroll
        for (int i = 0; i < NQ; ++i)
          #pragma unroll
          for (int ks = 0; ks < 4; ++ks) og1[i][ks] = lds_b128(L, (qA ^ (64u * ks)) + 4096u * i);
      }
      pj.piece(c, kq, L_W + ((kq + 1) & 1) * SLOT, (u32)(hh & 1) ^ dh0);
      issue_kv(c, c.srdV, s + 2, vdst, vv0, vv0, vv2, vv2);
      s3 = (s3 == 2) ? 0 : s3 + 1;
      TICK(2);
      barrier_vm<4>();
      TICK(4);
    }
  }
  #pragma unroll
  for (int i = 0; i < 4 * NQ; ++i) park[i] = bf16x8{};
  const __amdgpu_buffer_rsrc_t srdO = mksrd(p.wo[c.br], NH * DH * DM * 2);
  #pragma unroll 1
  for (u32 s = 24; s < 32; ++s) {
    const u32 vdst = L_V + ((s3 == 0) ? 2 : s3 - 1) * SLOT;
    if (s + 2 < 32) issue_kv(c, c.srdV, s + 2, vdst, vv0, vv0, vv2, vv2);
    if (s == WARMS) {
      #pragma unroll
      for (int i = 0; i < 8; ++i) dma_w(rfl(c.ldsb + L_W + 1024u * w + 4096u * i), 16u * ln, srdO, rfl((c.hg * 512u + 32u * b + 4u * i + w) * 1024u));
    }
    if (s == 25) {
      #pragma unroll
      for (int i = 0; i < NQ; ++i)
        #pragma unroll
        for (int ks = 0; ks < 4; ++ks) park[4 * i + ks] = lds_b128(L, (qA ^ (64u * ks)) + 4096u * i);
    }
    s3 = (s3 == 2) ? 0 : s3 + 1;
    if (s >= 30) barrier_vm<0>(); else if (s == WARMS || s == WARMS + 1) barrier_vm<12>(); else barrier_vm<4>();
  }
  STAMP(6);
  TICKOUT
  #pragma unroll
  for (int i = 0; i < NQ; ++i)
    #pragma unroll
    for (int ks = 0; ks < 4; ++ks) lds_w128(L, OREG + ((qA - L_QH) ^ (64u * ks)) + 4096u * i, og1[i][ks]);
}

__global__ __launch_bounds__(512, 2) void fused_attn(Params p) {
  extern __shared__ __attribute__((aligned(16))) char smem[];
  Ctx c;
  c.L = (LAS char*)smem;
  c.ldsb = (u32)(size_t)smem;
  c.wave = rfl(threadIdx.x >> 6);
  c.w = c.wave & 3;
  const u32 wave = c.wave, w = c.w;
  const u32 bid = blockIdx.x, xcd = bid & 7;
  c.j = bid >> 3; c.bid = bid;
  c.br = xcd & 1; c.hg = 2 * (xcd >> 1) + (c.j & 1); c.b = c.j >> 1;
  const size_t kvoff = (size_t)((c.b * NH + c.hg * 4) * SK) * DH * 2;
  c.srdK = mksrd((const char*)p.k[c.br] + kvoff, 4 * SK * DH * 2);
  c.srdV = mksrd((const char*)p.v[c.br] + kvoff, 4 * SK * DH * 2);
  STAMP(0); STAMPC(8);
  bf16x8 park[16];
  if (wave < 3) attention_role<2>(p, c, park);
  else if (wave == 3) attention_role<1>(p, c, park);
  else if (wave < 7) projection_role<2>(p, c, park);
  else projection_role<1>(p, c, park);

  {
    LAS char* L = c.L;
    const int ln = lane_id();
    const u32 cl = ln & 15, g = ln >> 4, q4 = cl >> 2, p4 = ln & 3;
    const __amdgpu_buffer_rsrc_t srdO = mksrd(p.wo[c.br], NH * DH * DM * 2);
    const u32 vo0 = 1024u * (ln >> 3) + 16u * ((ln & 7) ^ (2u * ((ln >> 4) & 1))), vo1 = 1024u * (ln >> 3) + 16u * ((ln & 7) ^ (2u * (((ln >> 4) & 1) + 2u)));
    const u32 ring = L_RING + wave * (3 * PIECE);
    auto issue_o = [&](u32 u, u32 slot3) {
      const u32 hs = (0x2013u >> (4u * ((u >> 2) & 3))) & 3u;
      const u32 so = (u < 16) ? ((c.hg * 4 + hs) * 128u + 32u * (u & 3)) * 1024u + 128u * wave : 0x01000000u;
      const u32 lb = c.ldsb + ring + slot3 * PIECE;
      dma_wo(rfl(lb), vo0, srdO, rfl(so));                     dma_wo(rfl(lb + 1024u), vo1, srdO, rfl(so + 8192u));
      dma_wo(rfl(lb + 2048u), vo0, srdO, rfl(so + 16384u));     dma_wo(rfl(lb + 3072u), vo1, srdO, rfl(so + 24576u));
    };
    const char* ackp = (const char*)p.ws + WS_ACK + 128u * member_bid(c, ln & 7) + 8u * c.hg;
    u64 ack;
    asm volatile("global_load_dwordx2 %0, %1, off sc1" : "=&v"(ack) : "v"(ackp) : "memory");
    issue_o(0, 0); issue_o(1, 1); issue_o(2, 2);
    barrier_lgkm();
    STAMP(7);
    const u32 oA = 256u * cl + 16u * (g ^ cl);
    const u32 wA = 128u * (8u * g + q4) + 16u * ((2u * ((q4 >> 1) + 2u * (g & 1))) | (p4 >> 1)) + 8u * (p4 & 1);
    auto aread = [&](bf16x8 (&af)[4], u32 slot3) {
      const u32 a0 = ring + slot3 * PIECE + wA;
      #pragma unroll
      for (int nt = 0; nt < 4; ++nt) { const u32 a = a0 ^ (32u * nt); af[nt] = lds_tr2(L, a, a + 512u); }
    };
    f32x4 acc[4][7];
    bf16x8 afA[4], afB[4], bq[7];
    auto oaddr = [&](u32 u) { return OREG * ((u >> 2) & 1) + (oA ^ (64u * (u & 3))); };
    SB();
    waitvm<8>();
    asm volatile("" : "+v"(ack));
    aread(afA, 0);
    {
      const u32 ob = oaddr(0);
      #pragma unroll
      for (int qt = 0; qt < 4; ++qt) bq[qt] = lds_b128(L, ob + 4096u * qt);
    }
    u32 sl = 0;
    auto kstep = [&](const u32 u, bf16x8 (&af)[4], bf16x8 (&afn)[4]) {
      const u32 sl1 = (sl == 2) ? 0 : sl + 1;
      const u32 ob = oaddr(u), obn = oaddr(u + 1);
      const bool pre = (u & 7) != 7;
      #pragma unroll
      for (int qt = 0; qt < 7; ++qt) {
        #pragma unroll
        for (int nt = 0; nt < 4; ++nt) { if (u == 0) MFMA_ZERO(acc[nt][qt], af[nt], bq[qt]); else MFMA_ACC(acc[nt][qt], af[nt], bq[qt]); }
        SB();
        if (qt < 3) bq[qt + 4] = lds_b128(L, ob + 4096u * (qt + 4));
        else if (pre) bq[qt - 3] = lds_b128(L, obn + 4096u * (qt - 3));
        if (qt == 0) issue_o(u + 3, sl);
        if (qt == 2) { waitvm<8>(); aread(afn, sl1); }
        SB();
      }
      sl = sl1;
    };
    #pragma unroll
    for (u32 u = 0; u < 16; u += 2) {
      if (u == 8) {
        barrier_lgkm();
        if (wave == 3 || wave == 7) {
          #pragma unroll
          for (int i = 0; i < 16; ++i) {
            if (wave == 3) lds_w128(L, (oA ^ (64u * (i & 3))) + 4096u * ((i >> 2) == 3 ? 6 : (i >> 2)), park[i]);
            else if (i >= 4) lds_w128(L, (oA ^ (64u * (i & 3))) + 4096u * (2 + (i >> 2)), park[i]);
          }
        }
        if (wave >= 4) {
          #pragma unroll
          for (int ks = 0; ks < 4; ++ks) {
            lds_w128(L, OREG + (oA ^ (64u * ks)) + 8192u * w, park[ks]);
            if (w < 3) lds_w128(L, OREG + (oA ^ (64u * ks)) + 8192u * w + 4096u, park[4 + ks]);
          }
        }
        barrier_lgkm();
        const u32 ob = oaddr(8);
        #pragma unroll
        for (int qt = 0; qt < 4; ++qt) bq[qt] = lds_b128(L, ob + 4096u * qt);
      }
      kstep(u, afA, afB);
      kstep(u + 1, afB, afA);
    }
    waitvm<0>();
    STAMP(11);
    NOP8();
    SB();
    const __amdgpu_buffer_rsrc_t srdP = mksrd((const char*)p.ws + WS_P + (size_t)(c.br * NB + c.b) * 8 * PART_WG, 8 * PART_WG);
    #pragma unroll
    for (int qt = 0; qt < 7; ++qt)
      #pragma unroll
      for (int np = 0; np < 2; ++np) {
        const u32x2 a = pack4n(acc[2 * np][qt]), bb = pack4n(acc[2 * np + 1][qt]);
        __builtin_amdgcn_raw_buffer_store_b128(u32x4{a[0], a[1], bb[0], bb[1]}, srdP, c.hg * PART_WG + wave * 14336u + (qt * 2 + np) * 1024u + ln * 16u, 0, 16);
      }
    waitvm<0>();
    barrier_lgkm();
    STAMP(11);
    if (wave == 0) {
      const u64 e0 = *lp<u64>(L_FLAG + 8u * (ln & 7));
      const u64 want = *lp<u64>(L_FLAG + 8u * c.hg) ^ ACK_MIX;
      for (u32 spin = 0; !__all(ack == want) && spin < SPIN_CAP; ++spin) { __builtin_amdgcn_s_sleep(8); ack = ld_agent(ackp); }
      if (ln == (int)c.hg) st_agent((char*)p.ws + WS_DONE + 128u * c.bid, e0 + 1);
      const char* dp = (const char*)p.ws + WS_DONE + 128u * member_bid(c, ln & 7);
      for (u32 spin = 0; spin < SPIN_CAP; ++spin) { if (__all(ld_agent(dp) == e0 + 1)) break; __builtin_amdgcn_s_sleep(2); }
    }
    barrier_lgkm();
    STAMP(12);
    if (wave < 7) {
      u32x4 x[2][8];
      #pragma unroll
      for (int np = 0; np < 2; ++np)
        #pragma unroll
        for (int m = 0; m < 8; ++m) x[np][m] = __builtin_amdgcn_raw_buffer_load_b128(srdP, m * PART_WG + c.hg * 14336u + (wave * 2 + np) * 1024u + ln * 16u, 0, 16);
      const u32 tile = wave * 2048u;
      #pragma unroll
      for (int np = 0; np < 2; ++np) {
        float s8[8] = {};
        #pragma unroll
        for (int m = 0; m < 8; ++m)
          #pragma unroll
          for (int i = 0; i < 4; ++i) { s8[2 * i] += __uint_as_float(x[np][m][i] << 16); s8[2 * i + 1] += __uint_as_float(x[np][m][i] & 0xffff0000u); }
        lds_w64(L, tile + cl * 128u + 64u * np + 8u * g, u32x2{cvtpk(s8[0], s8[1]), cvtpk(s8[2], s8[3])});
        lds_w64(L, tile + cl * 128u + 64u * np + 32u + 8u * g, u32x2{cvtpk(s8[4], s8[5]), cvtpk(s8[6], s8[7])});
      }
      #pragma unroll
      for (int i = 0; i < 2; ++i) {
        const u32 row = 8u * i + (ln >> 3), ch = ln & 7;
        char* o = (char*)p.out + (((size_t)c.b * SQ + 16u * wave + row) * (2 * DM) + c.br * DM + 64u * c.hg) * 2 + 16u * ch;
        *(bf16x8*)o = lds_b128(L, tile + row * 128u + 16u * ch);
      }
    }
  }
  STAMP(10); STAMPC(9);
  STAMPOUT();
}

extern "C" void kernel_launch(void* const* d_in, const int* in_sizes, int n_in, void* d_out, int out_size, void* d_ws, size_t ws_size,
                              hipStream_t stream) {
  Params p;
  for (int i = 0; i < 2; ++i) { p.x[i] = d_in[5 * i]; p.k[i] = d_in[5 * i + 1]; p.v[i] = d_in[5 * i + 2]; p.wq[i] = d_in[5 * i + 3]; p.wo[i] = d_in[5 * i + 4]; }
  p.out = d_out; p.ws = d_ws;
  (void)hipFuncSetAttribute((const void*)fused_attn, hipFuncAttributeMaxDynamicSharedMemorySize, LDS_BYTES);
  fused_attn<<<256, 512, LDS_BYTES, stream>>>(p);
}
```

```cpp
#include <hip/hip_runtime.h>
#include <hip/hip_bf16.h>
#include <type_traits>

typedef unsigned u32;
typedef unsigned long long u64;
typedef __bf16 bf16x8 __attribute__((ext_vector_type(8)));
typedef __bf16 bf16x4 __attribute__((ext_vector_type(4)));
typedef __bf16 bf16x2 __attribute__((ext_vector_type(2)));
typedef float f32x4 __attribute__((ext_vector_type(4)));
typedef float f32x2 __attribute__((ext_vector_type(2)));
typedef u32 u32x4 __attribute__((ext_vector_type(4)));
typedef u32 u32x2 __attribute__((ext_vector_type(2)));
#define LAS __attribute__((address_space(3)))
#define DI __device__ __forceinline__

constexpr int NB = 16, SQ = 112, DM = 512, NH = 32, SK = 512, DH = 128;
constexpr float QSCALE = 0.12751743082f;
constexpr float THR = 8.0f;

constexpr u32 SLOT = 16384;
constexpr u32 L_K = 0;
constexpr u32 L_V = 3 * SLOT;
constexpr u32 L_W = 6 * SLOT;
constexpr u32 L_QH = 8 * SLOT;
constexpr u32 LDS_BYTES = 10 * SLOT;
constexpr u32 OREG = 28672;
constexpr u32 PIECE = 4096;
constexpr u32 L_RS0 = 90112, L_RS1 = 122880, L_RS2 = 2 * OREG;

constexpr size_t WS_P = 0;
constexpr u32 PART_WG = 8 * 7 * 2 * 1024;
constexpr size_t WS_DONE = 32u << 20, WS_ACK = WS_DONE + 65536;
constexpr u32 L_FLAG = 159744;
constexpr unsigned long long ACK_MIX = 0x9E3779B97F4A7C15ull;
constexpr u32 SPIN_CAP = 1u << 18;

struct Params {
  const void* x[2]; const void* k[2]; const void* v[2]; const void* wq[2]; const void* wo[2];
  void* out; void* ws;
};

DI u32 rfl(u32 x) { return __builtin_amdgcn_readfirstlane(x); }
DI __amdgpu_buffer_rsrc_t mksrd(const void* p, u32 bytes) { return __builtin_amdgcn_make_buffer_rsrc((void*)p, (short)0, (int)bytes, 0x00020000); }
DI void dma_nt(u32 m0v, u32 voff, __amdgpu_buffer_rsrc_t srd, u32 soff) {
  asm volatile("s_mov_b32 m0, %0\n\ts_nop 0\n\tbuffer_load_dwordx4 %1, %2, %3 offen nt lds" :: "s"(m0v), "v"(voff), "s"(srd), "s"(soff) : "memory");
}
#define WPOL0 ""
#define WPOL1 ""
DI void dma_wo(u32 m0v, u32 voff, __amdgpu_buffer_rsrc_t srd, u32 soff) {
  asm volatile("s_mov_b32 m0, %0\n\ts_nop 0\n\tbuffer_load_dwordx4 %1, %2, %3 offen" WPOL1 " lds" :: "s"(m0v), "v"(voff), "s"(srd), "s"(soff) : "memory");
}
DI void dma_w(u32 m0v, u32 voff, __amdgpu_buffer_rsrc_t srd, u32 soff) {
  asm volatile("s_mov_b32 m0, %0\n\ts_nop 0\n\tbuffer_load_dwordx4 %1, %2, %3 offen" WPOL0 " lds" :: "s"(m0v), "v"(voff), "s"(srd), "s"(soff) : "memory");
}
#define KROTM 1
#define PF 3
#define PFP 6
#define WARMS 29
#define KPOS 1
#define KROT (KROTM == 1 ? (((b >> 2) + 2 * (j & 1)) & 3) : 0)
#define DROT (KROTM == 1 ? ((b >> 1) & 1) : 0)

template <int N> DI void waitvm() { asm volatile("s_waitcnt vmcnt(%0)" :: "i"(N) : "memory"); }
template <int N> DI void waitlgkm() { __builtin_amdgcn_s_waitcnt(0xC07F | (N << 8)); }
DI void barrier_lgkm() { asm volatile("" ::: "memory"); waitlgkm<0>(); __builtin_amdgcn_s_barrier(); asm volatile("" ::: "memory"); }
template <int N> DI void barrier_vm() { asm volatile("s_waitcnt vmcnt(%0)" :: "i"(N) : "memory"); waitlgkm<0>(); __builtin_amdgcn_s_barrier(); asm volatile("" ::: "memory"); }
DI u32 cvtpk(float lo, float hi) { f32x2 v = {lo, hi}; bf16x2 b = __builtin_convertvector(v, bf16x2); return __builtin_bit_cast(u32, b); }
DI u32x2 pack4(const f32x4& a, float s) { return u32x2{cvtpk(a[0] * s, a[1] * s), cvtpk(a[2] * s, a[3] * s)}; }
DI u32x2 pack4n(const f32x4& a) { return u32x2{cvtpk(a[0], a[1]), cvtpk(a[2], a[3])}; }
template <class T> DI LAS T* lp(u32 off) { return (LAS T*)off; }
DI bf16x8 lds_b128(LAS char*, u32 off) { return *lp<bf16x8>(off); }
DI void lds_w128(LAS char*, u32 off, bf16x8 v) { *lp<bf16x8>(off) = v; }
DI void lds_w64(LAS char*, u32 off, u32x2 v) { *lp<u32x2>(off) = v; }
DI bf16x8 lds_tr2(LAS char*, u32 off0, u32 off1) {
  bf16x4 a = __builtin_amdgcn_ds_read_tr16_b64_v4bf16(lp<bf16x4>(off0));
  bf16x4 b = __builtin_amdgcn_ds_read_tr16_b64_v4bf16(lp<bf16x4>(off1));
  return __builtin_shufflevector(a, b, 0, 1, 2, 3, 4, 5, 6, 7);
}
#define STAMP(i)
#define STAMPC(i)
#define STAMPOUT()
#define STAMPS(i)
#define SB() __builtin_amdgcn_sched_barrier(0)
#define TICK(i)
#define TICKDECL
#define TICKOUT
#define PIN(x) asm volatile("" : "+v"(x))
#define OPAQUE_S(x) asm volatile("" : "+s"(x))
DI float vmax3(float a, float b, float c) { return __builtin_elementwise_maximum(__builtin_elementwise_maximum(a, b), c); }
#define MFMA_ACC(acc, a, b) asm volatile("v_mfma_f32_16x16x32_bf16 %0, %1, %2, %0" : "+v"(acc) : "v"(a), "v"(b))
#define MFMA_INIT(acc, a, b, c) asm volatile("v_mfma_f32_16x16x32_bf16 %0, %1, %2, %3" : "=&v"(acc) : "v"(a), "v"(b), "v"(c))
#define MFMA_ZERO(acc, a, b) asm volatile("v_mfma_f32_16x16x32_bf16 %0, %1, %2, 0" : "=&v"(acc) : "v"(a), "v"(b))
#define NOP8() asm volatile("s_nop 7")
#define NOP2() asm volatile("s_nop 1")
#define EXP2(x) __builtin_amdgcn_exp2f(x)
#define GAS __attribute__((address_space(1)))
DI u64 ld_agent(const char* p) { return __hip_atomic_load((const GAS u64*)p, __ATOMIC_RELAXED, __HIP_MEMORY_SCOPE_AGENT); }
DI void st_agent(char* p, u64 v) { __hip_atomic_store((GAS u64*)p, v, __ATOMIC_RELAXED, __HIP_MEMORY_SCOPE_AGENT); }
DI int lane_id() { int ln; asm volatile("v_mbcnt_lo_u32_b32 %0, -1, 0\n\tv_mbcnt_hi_u32_b32 %0, -1, %0" : "=v"(ln)); return ln; }
DI float xg_max(float x) {
  auto a = __builtin_amdgcn_permlane16_swap(__float_as_uint(x), __float_as_uint(x), false, false);
  x = fmaxf(__uint_as_float(a[0]), __uint_as_float(a[1]));
  auto b = __builtin_amdgcn_permlane32_swap(__float_as_uint(x), __float_as_uint(x), false, false);
  return fmaxf(__uint_as_float(b[0]), __uint_as_float(b[1]));
}
DI float xg_sum(float x) {
  auto a = __builtin_amdgcn_permlane16_swap(__float_as_uint(x), __float_as_uint(x), false, false);
  x = __uint_as_float(a[0]) + __uint_as_float(a[1]);
  auto b = __builtin_amdgcn_permlane32_swap(__float_as_uint(x), __float_as_uint(x), false, false);
  return __uint_as_float(b[0]) + __uint_as_float(b[1]);
}

struct Ctx {
  LAS char* L; u32 ldsb, wave, w, br, b, hg, j, bid;
  __amdgpu_buffer_rsrc_t srdK, srdV;
};

DI u32 member_bid(const Ctx& c, u32 m) { return 8u * (2u * c.b + (m & 1u)) + c.br + 2u * (m >> 1); }

DI u32 ring_slot(u32 wave, u32 k) { return (k == 0 ? L_RS0 : k == 1 ? L_RS1 : L_RS2) + wave * PIECE; }
DI void issue_wo(const Params& p, const Ctx& c, int ln, u32 u, u32 k) {
  const __amdgpu_buffer_rsrc_t srdO = mksrd(p.wo[c.br], NH * DH * DM * 2);
  const u32 vo0 = 1024u * (ln >> 3) + 16u * ((ln & 7) ^ (2u * ((ln >> 4) & 1))), vo1 = vo0 ^ 64u;
  const u32 hs = (0x2013u >> (4u * ((u >> 2) & 3))) & 3u;
  const u32 so = (u < 16) ? ((c.hg * 4 + hs) * 128u + 32u * (u & 3)) * 1024u + 128u * c.wave : 0x01000000u;
  const u32 lb = c.ldsb + ring_slot(c.wave, k);
  dma_wo(rfl(lb), vo0, srdO, rfl(so));                     dma_wo(rfl(lb + 1024u), vo1, srdO, rfl(so + 8192u));
  dma_wo(rfl(lb + 2048u), vo0, srdO, rfl(so + 16384u));     dma_wo(rfl(lb + 3072u), vo1, srdO, rfl(so + 24576u));
}

DI void issue_kv(Ctx& c, const __amdgpu_buffer_rsrc_t srd, u32 st, u32 la, u32 vo0, u32 vo1, u32 vo2, u32 vo3) {
  asm volatile("s_mov_b32 m0, %0\n\ts_nop 0\n\t"
               "buffer_load_dwordx4 %1, %5, %6 offen nt lds\n\t"
               "buffer_load_dwordx4 %2, %5, %6 offen offset:1024 nt lds\n\t"
               "buffer_load_dwordx4 %3, %5, %6 offen offset:2048 nt lds\n\t"
               "buffer_load_dwordx4 %4, %5, %6 offen offset:3072 nt lds"
               :: "s"(rfl(c.ldsb + la + 4096u * c.w)), "v"(vo0), "v"(vo1), "v"(vo2), "v"(vo3), "s"(srd), "s"(rfl(st * SLOT + 4096u * c.w)) : "memory");
}

DI u32 wq_pattern(const Ctx& c, int ln) { return 8192u * (ln >> 3) + 16u * ((ln & 7) ^ (2u * (((ln >> 4) & 1) + 2u * (c.w & 1)))); }
DI void issue_wq(const Ctx& c, const __amdgpu_buffer_rsrc_t srdW, u32 vo_w, u32 rdn, u32 pi, u32 la) {
  const u32 b = c.b, j = c.j; (void)j; (void)b;
  const u32 hd = c.hg * 4 + rdn;
  const u32 kq = ((pi & 3) + KROT) & 3, dh = (pi >> 2) ^ DROT;
  const u32 so = (128u * kq + 8u * c.w) * 8192u + (128u * hd + 64u * dh) * 2u;
  #pragma unroll
  for (int i = 0; i < 4; ++i) dma_w(rfl(c.ldsb + la + 1024u * c.w + 4096u * i), vo_w, srdW, rfl(so + 32u * 8192u * i));
}
DI void issue_x_rows(const Params& p, Ctx& c, int ln) {
  const __amdgpu_buffer_rsrc_t srdX = mksrd((const char*)p.x[c.br] + (size_t)c.b * SQ * DM * 2, SQ * DM * 2);
  const u32 start = 56u * (c.j & 1);
  auto one = [&](u32 row) {
    row += start; row = row >= 112u ? row - 112u : row;
    dma_w(rfl(c.ldsb + 1024u * row), (16u * ln) ^ rfl(16u * (row & 15u)), srdX, rfl(1024u * row));
  };
  if (c.wave < 4) {
    #pragma unroll
    for (int i = 0; i < 16; ++i) one(c.w + 4u * i);
  } else {
    #pragma unroll
    for (int i = 0; i < 12; ++i) one(64u + c.w + 4u * i);
  }
}

template <int NQ> struct Proj {
  bf16x8 xf[NQ][16];
  f32x4 qa[4][NQ];
  u32 wA, rowq, oW;
  DI void init(const Ctx& c, int ln) {
    const u32 cl = ln & 15, g = ln >> 4, q4 = cl >> 2, p4 = ln & 3;
    wA = 128u * (8u * g + q4) + 16u * ((2u * ((q4 >> 1) + 2u * (g & 1))) | (p4 >> 1)) + 8u * (p4 & 1);
    rowq = L_QH + 256u * (32u * c.w + cl);
    oW = 16u * ((g >> 1) ^ cl) + 8u * (g & 1);
  }
  DI void read_x(const Ctx& c, int ln) {
    const u32 b = c.b, j = c.j; (void)j; (void)b;
    const u32 cl = ln & 15, g = ln >> 4;
    const u32 xA = 1024u * (32u * c.w + cl) + 16u * (g ^ cl);
    #pragma unroll
    for (int jq = 0; jq < 4; ++jq) {
      const u32 a = xA + 256u * (((u32)jq + KROT) & 3u);
      #pragma unroll
      for (int i = 0; i < NQ; ++i)
        #pragma unroll
        for (int ks = 0; ks < 4; ++ks) xf[i][4 * jq + ks] = lds_b128(c.L, (a ^ (64u * ks)) + 16384u * i);
    }
  }
  template <int PFV = PF>
  DI void piece(const Ctx& c, const int kq, u32 la, u32 dh) {
    LAS char* L = c.L;
    OPAQUE_S(la);
    u32 wa[4];
    #pragma unroll
    for (int d = 0; d < 4; ++d) wa[d] = la + (wA ^ (32u * d));
    auto wread = [&](const int n) { return lds_tr2(L, wa[n & 3] + 4096u * (n >> 2), wa[n & 3] + 4096u * (n >> 2) + 512u); };
    bf16x8 fr[16];
    #pragma unroll
    for (int n = 0; n < 2 * PFV; ++n) fr[n] = wread(n);
    SB();
    #pragma unroll
    for (int hp = 0; hp < 8; ++hp) {
      const int young = 4 * ((7 - hp) < (PFV - 1) ? (7 - hp) : (PFV - 1));
      if (young >= 12) waitlgkm<12>(); else if (young == 8) waitlgkm<8>(); else if (young == 4) waitlgkm<4>(); else waitlgkm<0>();
      SB();
      #pragma unroll
      for (int e = 0; e < 2; ++e) {
        const int n = 2 * hp + e, ks = n >> 2, d = n & 3;
        #pragma unroll
        for (int i = 0; i < NQ; ++i) { if (kq == 0 && ks == 0) MFMA_ZERO(qa[d][i], fr[n], xf[i][4 * kq + ks]); else MFMA_ACC(qa[d][i], fr[n], xf[i][4 * kq + ks]); }
      }
      SB();
      if (hp + PFV < 8) { fr[2 * (hp + PFV)] = wread(2 * (hp + PFV)); fr[2 * (hp + PFV) + 1] = wread(2 * (hp + PFV) + 1); }
      SB();
    }
    if (kq == 3) {
      NOP8();
      SB();
      #pragma unroll
      for (int i = 0; i < NQ; ++i)
        #pragma unroll
        for (int d = 0; d < 4; ++d) lds_w64(L, rowq + 4096u * i + (oW ^ (32u * (4u * dh + d))), pack4(qa[d][i], QSCALE));
    }
  }
};

template <int NQ>
DI void attention_role(const Params& p, Ctx& c, bf16x8 (&park)[16]) {
  LAS char* L = c.L;
  const u32 w = c.w, wave = c.wave; (void)wave;
  const int ln = lane_id();
  const u32 cl = ln & 15, g = ln >> 4, q4 = cl >> 2, p4 = ln & 3;
  const u32 pi_ = (cl & 3) | ((cl & 4) << 1) | ((cl & 8) >> 1);
  const u32 vk0 = 256u * g + 16u * (cl ^ g), vk1 = vk0 ^ 64u, vk2 = vk0 ^ 128u, vk3 = vk0 ^ 192u;
  const u32 kA = 256u * pi_ + 16u * (g ^ pi_);
  const u32 vA = 256u * (8u * (g & 1) + 4u * (g >> 1) + q4) + 16u * ((2u * (q4 + 4u * (g & 1))) | (p4 >> 1)) + 8u * (p4 & 1);
  const u32 rowq = L_QH + 256u * (32u * w + cl);
  const u32 qA = rowq + 16u * (g ^ cl);
  const u32 oW = 16u * ((g >> 1) ^ cl) + 8u * (g & 1);
  u64 d0 = 0;
  {
    const u32 b = c.b, j = c.j; (void)j; (void)b;
    const __amdgpu_buffer_rsrc_t srdW = mksrd(p.wq[c.br], DM * NH * DH * 2);
    const u32 vo_w = wq_pattern(c, ln);
    const u32 dh1 = DROT ^ 1u;
    Proj<NQ> pj;
    pj.init(c, ln);
    if (NQ == 1) d0 = ld_agent((const char*)p.ws + WS_DONE + 128u * member_bid(c, ln & 7));
    issue_x_rows(p, c, ln); issue_wq(c, srdW, vo_w, 0, 4, 8 * SLOT);
    barrier_vm<4>();
    if (NQ == 1) PIN(d0);
    pj.read_x(c, ln);
    barrier_vm<0>();
    issue_wq(c, srdW, vo_w, 0, 5, 3 * SLOT); issue_wq(c, srdW, vo_w, 0, 6, 4 * SLOT);
    pj.template piece<PFP>(c, 0, 8 * SLOT, dh1);
    barrier_vm<4>();
    issue_wq(c, srdW, vo_w, 0, 7, 2 * SLOT);
    pj.template piece<PFP>(c, 1, 3 * SLOT, dh1);
    issue_kv(c, c.srdK, 0, L_K, vk0, vk1, vk2, vk3);
    barrier_vm<8>();
    pj.template piece<PFP>(c, 2, 4 * SLOT, dh1);
    issue_kv(c, c.srdK, 1, L_K + SLOT, vk0, vk1, vk2, vk3);
    barrier_vm<8>();
    pj.template piece<PFP>(c, 3, 2 * SLOT, dh1);
    barrier_vm<4>();
  }
  issue_kv(c, c.srdK, 2, L_K + 2 * SLOT, vk0, vk1, vk2, vk3);
  bf16x8 kf[16];
  #pragma unroll
  for (int i = 0; i < 16; ++i) kf[i] = lds_b128(L, L_K + (kA ^ (64u * (i & 3))) + 4096u * (i >> 2));
  barrier_vm<4>();
  STAMP(1); STAMPS(0);
  bf16x8 qf[NQ][4];
  if (NQ == 1) {
    #pragma unroll
    for (int i = 0; i < 16; ++i) park[i] = bf16x8{};
  }
  bf16x8 ones = __builtin_bit_cast(bf16x8, u32x4{0x3F803F80u, 0x3F803F80u, 0x3F803F80u, 0x3F803F80u});
  PIN(ones);
  u32 s3 = 0;
  f32x4 O[8][NQ], negm[NQ], l[NQ];
  float m[NQ];
  #pragma unroll
  for (int i = 0; i < NQ; ++i) {
    m[i] = 0.f; negm[i] = f32x4{}; l[i] = f32x4{1.f, 1.f, 1.f, 1.f};
    #pragma unroll
    for (int d = 0; d < 8; ++d) O[d][i] = f32x4{};
  }
  TICKDECL
  auto step = [&](auto first_tag, const int rd, const int t) {
    constexpr bool FIRST = decltype(first_tag)::value;
    const u32 s3n = (s3 == 2) ? 0 : s3 + 1;
    const u32 vbase = L_V + s3 * SLOT, kbase = L_K + s3n * SLOT;
    const u32 sK = 8u * rd + t + 3;
    const bool kdma = FIRST || sK < 32;
    const u32 kdst = L_K + s3 * SLOT;
    TICK(0);
    float inv[NQ];
    if (FIRST) {
      #pragma unroll
      for (int i = 0; i < NQ; ++i)
        #pragma unroll
        for (int ks = 0; ks < 4; ++ks) qf[i][ks] = lds_b128(L, (qA ^ (64u * ks)) + 4096u * i);
      #pragma unroll
      for (int i = 0; i < NQ; ++i) inv[i] = __builtin_amdgcn_rcpf(l[i][0]);
      if (NQ == 1 && rd == 3) {
        if (ln < 8) { *lp<u64>(L_FLAG + 8u * ln) = d0; st_agent((char*)p.ws + WS_ACK + 128u * c.bid + 8u * ln, d0 ^ ACK_MIX); }
      }
    }
    if (KPOS == 0 && kdma) issue_kv(c, c.srdK, sK, kdst, vk0, vk1, vk2, vk3);
    if (!FIRST && sK == 34) { const int l2 = lane_id(); issue_wo(p, c, l2, 0, 0); issue_wo(p, c, l2, 1, 1); }
    TICK(1);
    if (!FIRST && NQ == 1 && rd == 1 && t == 1) {
      #pragma unroll
      for (int i = 0; i < 16; ++i) park[i] = lds_b128(L, ((L_QH + 256u * cl + 16u * (g ^ cl)) ^ (64u * (i & 3))) + 4096u * ((i >> 2) == 3 ? 6 : (i >> 2)));
    }
    u32 va[8];
    va[0] = vbase + vA;
    #pragma unroll
    for (int d = 1; d < 8; ++d) va[d] = va[0] ^ (32u * d);
    auto vread = [&](const int d, const int u) { return lds_tr2(L, va[d] + 8192u * u, va[d] + 8192u * u + 4096u); };
    if (FIRST) { waitlgkm<0>(); SB(); }
    f32x4 S[4][NQ];
    bf16x8 vf[16];
    #pragma unroll
    for (int kt = 0; kt < 2; ++kt)
      #pragma unroll
      for (int ks = 0; ks < 4; ++ks) {
        #pragma unroll
        for (int i = 0; i < NQ; ++i) { if (ks == 0) MFMA_INIT(S[kt][i], kf[4 * kt + ks], qf[i][ks], negm[i]); else MFMA_ACC(S[kt][i], kf[4 * kt + ks], qf[i][ks]); }
        vf[4 * kt + ks] = vread(4 * kt + ks, 0);
        if (FIRST) {
          const int d = 4 * kt + ks;
          #pragma unroll
          for (int i = 0; i < NQ; ++i) lds_w64(L, rowq + 4096u * i + (oW ^ (32u * d)), pack4(O[d][i], inv[i]));
        }
        SB();
      }
    u32x4 pw[NQ][2];
    float pa[NQ];
    #pragma unroll
    for (int kt = 2; kt < 4; ++kt)
      #pragma unroll
      for (int ks = 0; ks < 4; ++ks) {
        const int n = 4 * (kt - 2) + ks;
        #pragma unroll
        for (int i = 0; i < NQ; ++i) { if (ks == 0) MFMA_INIT(S[kt][i], kf[4 * kt + ks], qf[i][ks], negm[i]); else MFMA_ACC(S[kt][i], kf[4 * kt + ks], qf[i][ks]); }
        vf[8 + n] = vread(n, 1);
        if (n >= 2 && n < 6) {
          const int k2 = (n - 2) >> 1, e = 2 * ((n - 2) & 1);
          #pragma unroll
          for (int i = 0; i < NQ; ++i) {
            const float x0 = S[k2][i][e], x1 = S[k2][i][e + 1];
            pa[i] = (n == 2) ? __builtin_elementwise_maximum(x0, x1) : vmax3(pa[i], x0, x1);
            u32 pk = cvtpk(EXP2(x0), EXP2(x1)); PIN(pk);
            pw[i][0][2 * k2 + (e >> 1)] = pk;
          }
        }
        SB();
      }
    asm volatile("" ::: "memory");
    NOP8();
    SB();
    TICK(2);
    #pragma unroll
    for (int i = 0; i < NQ; ++i) {
      const float pb = vmax3(S[2][i][0], S[2][i][1], S[2][i][2]);
      pa[i] = vmax3(pa[i], S[2][i][3], S[3][i][0]); pa[i] = vmax3(pa[i], S[3][i][1], S[3][i][2]);
      pa[i] = vmax3(pa[i], pb, S[3][i][3]);
    }
    bool move;
    if (FIRST) {
      move = false;
      #pragma unroll
      for (int i = 0; i < NQ; ++i) { pa[i] = xg_max(pa[i]); move |= !(__builtin_fabsf(pa[i]) <= THR); }
    } else move = (NQ == 2 ? __builtin_elementwise_maximum(pa[0], pa[NQ - 1]) : pa[0]) > THR;
    if (__builtin_expect(__any(move), 0)) {
      #pragma unroll
      for (int i = 0; i < NQ; ++i) {
        const float delta = FIRST ? pa[i] : fmaxf(xg_max(pa[i]), 0.f);
        m[i] += delta;
        negm[i] = f32x4{-m[i], -m[i], -m[i], -m[i]};
        #pragma unroll
        for (int kt = 0; kt < 4; ++kt) S[kt][i] -= delta;
        #pragma unroll
        for (int k2 = 0; k2 < 2; ++k2)
          #pragma unroll
          for (int e = 0; e < 4; e += 2) pw[i][0][2 * k2 + (e >> 1)] = cvtpk(EXP2(S[k2][i][e]), EXP2(S[k2][i][e + 1]));
        if (!FIRST) {
          const float alpha = EXP2(-delta);
          l[i] *= alpha;
          #pragma unroll
          for (int d = 0; d < 8; ++d) O[d][i] *= alpha;
        }
      }
      SB();
      NOP2();
    }
    if (KPOS == 1 && kdma) issue_kv(c, c.srdK, sK, kdst, vk0, vk1, vk2, vk3);
    waitlgkm<15>();
    SB();
    TICK(3);
    #pragma unroll
    for (int i = 0; i < NQ; ++i) { if (FIRST) MFMA_ZERO(l[i], ones, __builtin_bit_cast(bf16x8, pw[i][0])); else MFMA_ACC(l[i], ones, __builtin_bit_cast(bf16x8, pw[i][0])); }
    #pragma unroll
    for (int d = 0; d < 8; ++d) {
      #pragma unroll
      for (int i = 0; i < NQ; ++i) { if (FIRST) MFMA_ZERO(O[d][i], vf[d], __builtin_bit_cast(bf16x8, pw[i][0])); else MFMA_ACC(O[d][i], vf[d], __builtin_bit_cast(bf16x8, pw[i][0])); }
      if (d < 4) {
        const int k2 = 2 + (d >> 1), e = 2 * (d & 1);
        #pragma unroll
        for (int i = 0; i < NQ; ++i) { u32 pk = cvtpk(EXP2(S[k2][i][e]), EXP2(S[k2][i][e + 1])); PIN(pk); pw[i][1][2 * (k2 - 2) + (e >> 1)] = pk; }
      }
      SB();
    }
    u32 ka[4];
    ka[0] = kbase + kA;
    #pragma unroll
    for (int ks = 1; ks < 4; ++ks) ka[ks] = ka[0] ^ (64u * ks);
    waitlgkm<0>();
    SB();
    #pragma unroll
    for (int i = 0; i < NQ; ++i) MFMA_ACC(l[i], ones, __builtin_bit_cast(bf16x8, pw[i][1]));
    #pragma unroll
    for (int d = 0; d < 8; ++d) {
      #pragma unroll
      for (int i = 0; i < NQ; ++i) MFMA_ACC(O[d][i], vf[8 + d], __builtin_bit_cast(bf16x8, pw[i][1]));
      SB();
      kf[2 * d] = lds_b128(L, ka[(2 * d) & 3] + 4096u * ((2 * d) >> 2));
      kf[2 * d + 1] = lds_b128(L, ka[(2 * d + 1) & 3] + 4096u * ((2 * d + 1) >> 2));
      SB();
    }
    s3 = s3n;
    TICK(4);
    if (FIRST && NQ == 1 && rd == 3) barrier_vm<5>();
    else if (kdma) barrier_vm<4>(); else barrier_vm<0>();
    TICK(5);
    STAMPS(8 * rd + t + 1);
  };
  #pragma unroll 1
  for (int rd = 0; rd < 4; ++rd) {
    step(std::true_type{}, rd, 0);
    #pragma unroll 1
    for (int t = 1; t < 8; ++t) step(std::false_type{}, rd, t);
    STAMP(2 + rd);
  }
  NOP8();
  SB();
  #pragma unroll
  for (int i = 0; i < NQ; ++i) {
    const float inv = __builtin_amdgcn_rcpf(l[i][0]);
    #pragma unroll
    for (int d = 0; d < 8; ++d) lds_w64(L, rowq - L_QH + 4096u * i + (oW ^ (32u * d)), pack4(O[d][i], inv));
  }
  TICKOUT
}

template <int NQ>
DI void projection_role(const Params& p, Ctx& c, bf16x8 (&park)[16]) {
  LAS char* L = c.L;
  const u32 w = c.w, wave = c.wave; (void)wave;
  const u32 b = c.b, j = c.j; (void)j;
  const int ln = lane_id();
  const u32 cl = ln & 15, g = ln >> 4, q4 = cl >> 2, p4 = ln & 3;
  const u32 vv0 = 256u * g + 16u * (cl ^ (2u * g)), vv2 = vv0 ^ 128u;
  const __amdgpu_buffer_rsrc_t srdW = mksrd(p.wq[c.br], DM * NH * DH * 2);
  const u32 rot = KROT, dh0 = DROT;
  const u32 vo_w = wq_pattern(c, ln);
  auto issue_w = [&](u32 rdn, u32 pi, u32 la) { issue_wq(c, srdW, vo_w, rdn, pi, la); };
  Proj<NQ> pj;
  pj.init(c, ln);
  const u32 rowq = pj.rowq;
  const u32 qA = rowq + 16u * (g ^ cl);
  STAMP(13);
  issue_w(0, 0, 7 * SLOT); issue_x_rows(p, c, ln); issue_w(0, 1, 9 * SLOT);
  STAMP(14);
  barrier_vm<4>();
  STAMP(1);
  pj.read_x(c, ln);
  barrier_lgkm();
  issue_w(0, 2, 5 * SLOT);
  pj.template piece<PFP>(c, 0, 7 * SLOT, dh0);
  barrier_vm<4>();
  STAMP(2);
  issue_w(0, 3, 6 * SLOT);
  pj.template piece<PFP>(c, 1, 9 * SLOT, dh0);
  barrier_vm<4>();
  issue_w(1, 0, 7 * SLOT);
  pj.template piece<PFP>(c, 2, 5 * SLOT, dh0);
  issue_kv(c, c.srdV, 0, L_V, vv0, vv0, vv2, vv2);
  barrier_vm<8>();
  STAMP(3);
  issue_kv(c, c.srdV, 1, L_V + SLOT, vv0, vv0, vv2, vv2);
  pj.template piece<PFP>(c, 3, 6 * SLOT, dh0);
  barrier_lgkm();
  STAMP(4);
  barrier_vm<4>();
  STAMP(5);
  bf16x8 og1[NQ][4];
  #pragma unroll
  for (int i = 0; i < NQ; ++i)
    #pragma unroll
    for (int ks = 0; ks < 4; ++ks) og1[i][ks] = bf16x8{};
  if (NQ == 1) {
    #pragma unroll
    for (int i = 4; i < 16; ++i) park[i] = bf16x8{};
  }
  u32 s3 = 0;
  TICKDECL
  #pragma unroll 1
  for (int hh = 0; hh < 6; ++hh) {
    #pragma unroll
    for (int kq = 0; kq < 4; ++kq) {
      const u32 s = 4u * hh + kq, s1 = s + 1;
      const u32 vdst = L_V + ((s3 == 0) ? 2 : s3 - 1) * SLOT;
      TICK(0);
      issue_w((1 + (s1 >> 3)) & 3, s1 & 7, L_W + (kq & 1) * SLOT);
      TICK(1);
      if (NQ == 1 && kq == 1 && hh == 2) {
        #pragma unroll
        for (int i = 4; i < 16; ++i) park[i] = lds_b128(L, ((L_QH + 256u * cl + 16u * (g ^ cl)) ^ (64u * (i & 3))) + 4096u * (2 + (i >> 2)));
      }
      if (kq == 1 && hh == 4) {
        #pragma unroll
        for (int i = 0; i < NQ; ++i)
          #pragma unroll
          for (int ks = 0; ks < 4; ++ks) og1[i][ks] = lds_b128(L, (qA ^ (64u * ks)) + 4096u * i);
      }
      pj.piece(c, kq, L_W + ((kq + 1) & 1) * SLOT, (u32)(hh & 1) ^ dh0);
      issue_kv(c, c.srdV, s + 2, vdst, vv0, vv0, vv2, vv2);
      s3 = (s3 == 2) ? 0 : s3 + 1;
      TICK(2);
      barrier_vm<4>();
      TICK(4);
    }
  }
  #pragma unroll
  for (int i = 0; i < 4 * NQ; ++i) park[i] = bf16x8{};
  const __amdgpu_buffer_rsrc_t srdO = mksrd(p.wo[c.br], NH * DH * DM * 2);
  #pragma unroll 1
  for (u32 s = 24; s < 32; ++s) {
    const u32 vdst = L_V + ((s3 == 0) ? 2 : s3 - 1) * SLOT;
    if (s + 2 < 32) issue_kv(c, c.srdV, s + 2, vdst, vv0, vv0, vv2, vv2);
    if (s == WARMS) {
      #pragma unroll
      for (int i = 0; i < 8; ++i) dma_w(rfl(c.ldsb + L_W + 1024u * w + 4096u * i), 16u * ln, srdO, rfl((c.hg * 512u + 32u * b + 4u * i + w) * 1024u));
    }
    if (s == 31) { issue_wo(p, c, ln, 0, 0); issue_wo(p, c, ln, 1, 1); }
    if (s == 25) {
      #pragma unroll
      for (int i = 0; i < NQ; ++i)
        #pragma unroll
        for (int ks = 0; ks < 4; ++ks) park[4 * i + ks] = lds_b128(L, (qA ^ (64u * ks)) + 4096u * i);
    }
    s3 = (s3 == 2) ? 0 : s3 + 1;
    if (s >= 30) barrier_vm<0>(); else if (s == WARMS || s == WARMS + 1) barrier_vm<12>(); else barrier_vm<4>();
  }
  STAMP(6);
  TICKOUT
  #pragma unroll
  for (int i = 0; i < NQ; ++i)
    #pragma unroll
    for (int ks = 0; ks < 4; ++ks) lds_w128(L, OREG + ((qA - L_QH) ^ (64u * ks)) + 4096u * i, og1[i][ks]);
}

__global__ __launch_bounds__(512, 2) void fused_attn(Params p) {
  extern __shared__ __attribute__((aligned(16))) char smem[];
  Ctx c;
  c.L = (LAS char*)smem;
  c.ldsb = (u32)(size_t)smem;
  c.wave = rfl(threadIdx.x >> 6);
  c.w = c.wave & 3;
  const u32 wave = c.wave, w = c.w;
  const u32 bid = blockIdx.x, xcd = bid & 7;
  c.j = bid >> 3; c.bid = bid;
  c.br = xcd & 1; c.hg = 2 * (xcd >> 1) + (c.j & 1); c.b = c.j >> 1;
  const size_t kvoff = (size_t)((c.b * NH + c.hg * 4) * SK) * DH * 2;
  c.srdK = mksrd((const char*)p.k[c.br] + kvoff, 4 * SK * DH * 2);
  c.srdV = mksrd((const char*)p.v[c.br] + kvoff, 4 * SK * DH * 2);
  STAMP(0); STAMPC(8);
  bf16x8 park[16];
  if (wave < 3) attention_role<2>(p, c, park);
  else if (wave == 3) attention_role<1>(p, c, park);
  else if (wave < 7) projection_role<2>(p, c, park);
  else projection_role<1>(p, c, park);

  {
    LAS char* L = c.L;
    const int ln = lane_id();
    const u32 cl = ln & 15, g = ln >> 4, q4 = cl >> 2, p4 = ln & 3;
    const char* ackp = (const char*)p.ws + WS_ACK + 128u * member_bid(c, ln & 7) + 8u * c.hg;
    u64 ack;
    asm volatile("global_load_dwordx2 %0, %1, off sc1" : "=&v"(ack) : "v"(ackp) : "memory");
    issue_wo(p, c, ln, 2, 2);
    barrier_lgkm();
    STAMP(7);
    const u32 oA = 256u * cl + 16u * (g ^ cl);
    const u32 wA = 128u * (8u * g + q4) + 16u * ((2u * ((q4 >> 1) + 2u * (g & 1))) | (p4 >> 1)) + 8u * (p4 & 1);
    auto aread = [&](bf16x8 (&af)[4], u32 slot3) {
      u32 rb = rfl(ring_slot(wave, slot3));
      OPAQUE_S(rb);
      const u32 a0 = rb + wA;
      #pragma unroll
      for (int nt = 0; nt < 4; ++nt) { const u32 a = a0 ^ (32u * nt); af[nt] = lds_tr2(L, a, a + 512u); }
    };
    f32x4 acc[4][7];
    bf16x8 afA[4], afB[4], bq[7];
    auto oaddr = [&](u32 u) { return OREG * ((u >> 2) & 1) + (oA ^ (64u * (u & 3))); };
    SB();
    aread(afA, 0);
    {
      const u32 ob = oaddr(0);
      #pragma unroll
      for (int qt = 0; qt < 4; ++qt) bq[qt] = lds_b128(L, ob + 4096u * qt);
    }
    u32 sl = 0;
    auto kstep = [&](const u32 u, bf16x8 (&af)[4], bf16x8 (&afn)[4]) {
      const u32 sl1 = (sl == 2) ? 0 : sl + 1;
      const u32 ob = oaddr(u), obn = oaddr(u + 1);
      const bool pre = (u & 7) != 7;
      #pragma unroll
      for (int qt = 0; qt < 7; ++qt) {
        #pragma unroll
        for (int nt = 0; nt < 4; ++nt) { if (u == 0) MFMA_ZERO(acc[nt][qt], af[nt], bq[qt]); else MFMA_ACC(acc[nt][qt], af[nt], bq[qt]); }
        SB();
        if (qt < 3) bq[qt + 4] = lds_b128(L, ob + 4096u * (qt + 4));
        else if (pre) bq[qt - 3] = lds_b128(L, obn + 4096u * (qt - 3));
        if (qt == 0) issue_wo(p, c, ln, u + 3, sl);
        if (qt == 2) { waitvm<8>(); if (u == 0) asm volatile("" : "+v"(ack)); aread(afn, sl1); }
        SB();
      }
      sl = sl1;
    };
    #pragma unroll
    for (u32 u = 0; u < 16; u += 2) {
      if (u == 8) {
        barrier_lgkm();
        if (wave == 3 || wave == 7) {
          #pragma unroll
          for (int i = 0; i < 16; ++i) {
            if (wave == 3) lds_w128(L, (oA ^ (64u * (i & 3))) + 4096u * ((i >> 2) == 3 ? 6 : (i >> 2)), park[i]);
            else if (i >= 4) lds_w128(L, (oA ^ (64u * (i & 3))) + 4096u * (2 + (i >> 2)), park[i]);
          }
        }
        if (wave >= 4) {
          #pragma unroll
          for (int ks = 0; ks < 4; ++ks) {
            lds_w128(L, OREG + (oA ^ (64u * ks)) + 8192u * w, park[ks]);
            if (w < 3) lds_w128(L, OREG + (oA ^ (64u * ks)) + 8192u * w + 4096u, park[4 + ks]);
          }
        }
        barrier_lgkm();
        const u32 ob = oaddr(8);
        #pragma unroll
        for (int qt = 0; qt < 4; ++qt) bq[qt] = lds_b128(L, ob + 4096u * qt);
      }
      kstep(u, afA, afB);
      kstep(u + 1, afB, afA);
    }
    waitvm<0>();
    STAMP(11);
    NOP8();
    SB();
    const __amdgpu_buffer_rsrc_t srdP = mksrd((const char*)p.ws + WS_P + (size_t)(c.br * NB + c.b) * 8 * PART_WG, 8 * PART_WG);
    #pragma unroll
    for (int qt = 0; qt < 7; ++qt)
      #pragma unroll
      for (int np = 0; np < 2; ++np) {
        const u32x2 a = pack4n(acc[2 * np][qt]), bb = pack4n(acc[2 * np + 1][qt]);
        __builtin_amdgcn_raw_buffer_store_b128(u32x4{a[0], a[1], bb[0], bb[1]}, srdP, c.hg * PART_WG + wave * 14336u + (qt * 2 + np) * 1024u + ln * 16u, 0, 16);
      }
    waitvm<0>();
    barrier_lgkm();
    STAMP(11);
    if (wave == 0) {
      const u64 e0 = *lp<u64>(L_FLAG + 8u * (ln & 7));
      const u64 want = *lp<u64>(L_FLAG + 8u * c.hg) ^ ACK_MIX;
      for (u32 spin = 0; !__all(ack == want) && spin < SPIN_CAP; ++spin) { __builtin_amdgcn_s_sleep(8); ack = ld_agent(ackp); }
      if (ln == (int)c.hg) st_agent((char*)p.ws + WS_DONE + 128u * c.bid, e0 + 1);
      const char* dp = (const char*)p.ws + WS_DONE + 128u * member_bid(c, ln & 7);
      for (u32 spin = 0; spin < SPIN_CAP; ++spin) { if (__all(ld_agent(dp) == e0 + 1)) break; __builtin_amdgcn_s_sleep(2); }
    }
    barrier_lgkm();
    STAMP(12);
    if (wave < 7) {
      u32x4 x[2][8];
      #pragma unroll
      for (int np = 0; np < 2; ++np)
        #pragma unroll
        for (int m = 0; m < 8; ++m) x[np][m] = __builtin_amdgcn_raw_buffer_load_b128(srdP, m * PART_WG + c.hg * 14336u + (wave * 2 + np) * 1024u + ln * 16u, 0, 16);
      const u32 tile = ring_slot(wave, 0);
      #pragma unroll
      for (int np = 0; np < 2; ++np) {
        float s8[8] = {};
        #pragma unroll
        for (int m = 0; m < 8; ++m)
          #pragma unroll
          for (int i = 0; i < 4; ++i) { s8[2 * i] += __uint_as_float(x[np][m][i] << 16); s8[2 * i + 1] += __uint_as_float(x[np][m][i] & 0xffff0000u); }
        lds_w64(L, tile + cl * 128u + 64u * np + 8u * g, u32x2{cvtpk(s8[0], s8[1]), cvtpk(s8[2], s8[3])});
        lds_w64(L, tile + cl * 128u + 64u * np + 32u + 8u * g, u32x2{cvtpk(s8[4], s8[5]), cvtpk(s8[6], s8[7])});
      }
      #pragma unroll
      for (int i = 0; i < 2; ++i) {
        const u32 row = 8u * i + (ln >> 3), ch = ln & 7;
        char* o = (char*)p.out + (((size_t)c.b * SQ + 16u * wave + row) * (2 * DM) + c.br * DM + 64u * c.hg) * 2 + 16u * ch;
        *(bf16x8*)o = lds_b128(L, tile + row * 128u + 16u * ch);
      }
    }
  }
  STAMP(10); STAMPC(9);
  STAMPOUT();
}

extern "C" void kernel_launch(void* const* d_in, const int* in_sizes, int n_in, void* d_out, int out_size, void* d_ws, size_t ws_size,
                              hipStream_t stream) {
  Params p;
  for (int i = 0; i < 2; ++i) { p.x[i] = d_in[5 * i]; p.k[i] = d_in[5 * i + 1]; p.v[i] = d_in[5 * i + 2]; p.wq[i] = d_in[5 * i + 3]; p.wo[i] = d_in[5 * i + 4]; }
  p.out = d_out; p.ws = d_ws;
  (void)hipFuncSetAttribute((const void*)fused_attn, hipFuncAttributeMaxDynamicSharedMemorySize, LDS_BYTES);
  fused_attn<<<256, 512, LDS_BYTES, stream>>>(p);
}
```

```cpp
#define OUT_NT
#include <hip/hip_runtime.h>
#include <hip/hip_bf16.h>
#include <type_traits>

typedef unsigned u32;
typedef unsigned long long u64;
typedef __bf16 bf16x8 __attribute__((ext_vector_type(8)));
typedef __bf16 bf16x4 __attribute__((ext_vector_type(4)));
typedef __bf16 bf16x2 __attribute__((ext_vector_type(2)));
typedef float f32x4 __attribute__((ext_vector_type(4)));
typedef float f32x2 __attribute__((ext_vector_type(2)));
typedef u32 u32x4 __attribute__((ext_vector_type(4)));
typedef u32 u32x2 __attribute__((ext_vector_type(2)));
#define LAS __attribute__((address_space(3)))
#define DI __device__ __forceinline__

constexpr int NB = 16, SQ = 112, DM = 512, NH = 32, SK = 512, DH = 128;
constexpr float QSCALE = 0.12751743082f;
constexpr float THR = 8.0f;

constexpr u32 SLOT = 16384;
constexpr u32 L_K = 0;
constexpr u32 L_V = 3 * SLOT;
constexpr u32 L_W = 6 * SLOT;
constexpr u32 L_QH = 8 * SLOT;
constexpr u32 LDS_BYTES = 10 * SLOT;
constexpr u32 OREG = 28672;
constexpr u32 PIECE = 4096;
constexpr u32 L_RS0 = 90112, L_RS1 = 122880, L_RS2 = 2 * OREG;

constexpr size_t WS_P = 0;
constexpr u32 PART_WG = 8 * 7 * 2 * 1024;
constexpr size_t WS_DONE = 32u << 20, WS_ACK = WS_DONE + 65536;
constexpr u32 L_FLAG = 159744;
constexpr unsigned long long ACK_MIX = 0x9E3779B97F4A7C15ull;
constexpr u32 SPIN_CAP = 1u << 18;

struct Params {
  const void* x[2]; const void* k[2]; const void* v[2]; const void* wq[2]; const void* wo[2];
  void* out; void* ws;
};

DI u32 rfl(u32 x) { return __builtin_amdgcn_readfirstlane(x); }
DI __amdgpu_buffer_rsrc_t mksrd(const void* p, u32 bytes) { return __builtin_amdgcn_make_buffer_rsrc((void*)p, (short)0, (int)bytes, 0x00020000); }
DI void dma_nt(u32 m0v, u32 voff, __amdgpu_buffer_rsrc_t srd, u32 soff) {
  asm volatile("s_mov_b32 m0, %0\n\ts_nop 0\n\tbuffer_load_dwordx4 %1, %2, %3 offen nt lds" :: "s"(m0v), "v"(voff), "s"(srd), "s"(soff) : "memory");
}
#define KVPOL " nt"
#define WPOL0 ""
#define WPOL1 ""
DI void dma_wo(u32 m0v, u32 voff, __amdgpu_buffer_rsrc_t srd, u32 soff) {
  asm volatile("s_mov_b32 m0, %0\n\ts_nop 0\n\tbuffer_load_dwordx4 %1, %2, %3 offen" WPOL1 " lds" :: "s"(m0v), "v"(voff), "s"(srd), "s"(soff) : "memory");
}
DI void dma_w(u32 m0v, u32 voff, __amdgpu_buffer_rsrc_t srd, u32 soff) {
  asm volatile("s_mov_b32 m0, %0\n\ts_nop 0\n\tbuffer_load_dwordx4 %1, %2, %3 offen" WPOL0 " lds" :: "s"(m0v), "v"(voff), "s"(srd), "s"(soff) : "memory");
}
#define KROTM 1
#define PF 3
#define PFP 6
#define WARMS 29
#define KPOS 1
#define KROT (KROTM == 1 ? (((b >> 2) + 2 * (j & 1)) & 3) : 0)
#define DROT (KROTM == 1 ? ((b >> 1) & 1) : 0)

template <int N> DI void waitvm() { asm volatile("s_waitcnt vmcnt(%0)" :: "i"(N) : "memory"); }
template <int N> DI void waitlgkm() { __builtin_amdgcn_s_waitcnt(0xC07F | (N << 8)); }
DI void barrier_lgkm() { asm volatile("" ::: "memory"); waitlgkm<0>(); __builtin_amdgcn_s_barrier(); asm volatile("" ::: "memory"); }
template <int N> DI void barrier_vm() { asm volatile("s_waitcnt vmcnt(%0)" :: "i"(N) : "memory"); waitlgkm<0>(); __builtin_amdgcn_s_barrier(); asm volatile("" ::: "memory"); }
DI u32 cvtpk(float lo, float hi) { f32x2 v = {lo, hi}; bf16x2 b = __builtin_convertvector(v, bf16x2); return __builtin_bit_cast(u32, b); }
DI u32x2 pack4(const f32x4& a, float s) { return u32x2{cvtpk(a[0] * s, a[1] * s), cvtpk(a[2] * s, a[3] * s)}; }
DI u32x2 pack4n(const f32x4& a) { return u32x2{cvtpk(a[0], a[1]), cvtpk(a[2], a[3])}; }
template <class T> DI LAS T* lp(u32 off) { return (LAS T*)off; }
DI bf16x8 lds_b128(LAS char*, u32 off) { return *lp<bf16x8>(off); }
DI void lds_w128(LAS char*, u32 off, bf16x8 v) { *lp<bf16x8>(off) = v; }
DI void lds_w64(LAS char*, u32 off, u32x2 v) { *lp<u32x2>(off) = v; }
DI bf16x8 lds_tr2(LAS char*, u32 off0, u32 off1) {
  bf16x4 a = __builtin_amdgcn_ds_read_tr16_b64_v4bf16(lp<bf16x4>(off0));
  bf16x4 b = __builtin_amdgcn_ds_read_tr16_b64_v4bf16(lp<bf16x4>(off1));
  return __builtin_shufflevector(a, b, 0, 1, 2, 3, 4, 5, 6, 7);
}
#define STAMP(i)
#define STAMPC(i)
#define STAMPOUT()
#define STAMPS(i)
#define SB() __builtin_amdgcn_sched_barrier(0)
#define TICK(i)
#define TICKDECL
#define TICKOUT
#define PIN(x) asm volatile("" : "+v"(x))
#define OPAQUE_S(x) asm volatile("" : "+s"(x))
DI float vmax3(float a, float b, float c) { return __builtin_elementwise_maximum(__builtin_elementwise_maximum(a, b), c); }
#define MFMA_ACC(acc, a, b) asm volatile("v_mfma_f32_16x16x32_bf16 %0, %1, %2, %0" : "+v"(acc) : "v"(a), "v"(b))
#define MFMA_INIT(acc, a, b, c) asm volatile("v_mfma_f32_16x16x32_bf16 %0, %1, %2, %3" : "=&v"(acc) : "v"(a), "v"(b), "v"(c))
#define MFMA_ZERO(acc, a, b) asm volatile("v_mfma_f32_16x16x32_bf16 %0, %1, %2, 0" : "=&v"(acc) : "v"(a), "v"(b))
#define NOP8() asm volatile("s_nop 7")
#define NOP2() asm volatile("s_nop 1")
#define EXP2(x) __builtin_amdgcn_exp2f(x)
#define GAS __attribute__((address_space(1)))
DI u64 ld_agent(const char* p) { return __hip_atomic_load((const GAS u64*)p, __ATOMIC_RELAXED, __HIP_MEMORY_SCOPE_AGENT); }
DI void st_agent(char* p, u64 v) { __hip_atomic_store((GAS u64*)p, v, __ATOMIC_RELAXED, __HIP_MEMORY_SCOPE_AGENT); }
DI int lane_id() { int ln; asm volatile("v_mbcnt_lo_u32_b32 %0, -1, 0\n\tv_mbcnt_hi_u32_b32 %0, -1, %0" : "=v"(ln)); return ln; }
DI float xg_max(float x) {
  auto a = __builtin_amdgcn_permlane16_swap(__float_as_uint(x), __float_as_uint(x), false, false);
  x = fmaxf(__uint_as_float(a[0]), __uint_as_float(a[1]));
  auto b = __builtin_amdgcn_permlane32_swap(__float_as_uint(x), __float_as_uint(x), false, false);
  return fmaxf(__uint_as_float(b[0]), __uint_as_float(b[1]));
}
DI float xg_sum(float x) {
  auto a = __builtin_amdgcn_permlane16_swap(__float_as_uint(x), __float_as_uint(x), false, false);
  x = __uint_as_float(a[0]) + __uint_as_float(a[1]);
  auto b = __builtin_amdgcn_permlane32_swap(__float_as_uint(x), __float_as_uint(x), false, false);
  return __uint_as_float(b[0]) + __uint_as_float(b[1]);
}

struct Ctx {
  LAS char* L; u32 ldsb, wave, w, br, b, hg, j, bid;
  __amdgpu_buffer_rsrc_t srdK, srdV;
};

DI u32 member_bid(const Ctx& c, u32 m) { return 8u * (2u * c.b + (m & 1u)) + c.br + 2u * (m >> 1); }

DI u32 ring_slot(u32 wave, u32 k) { return (k == 0 ? L_RS0 : k == 1 ? L_RS1 : L_RS2) + wave * PIECE; }
DI void issue_wo(const Params& p, const Ctx& c, int ln, u32 u, u32 k) {
  const __amdgpu_buffer_rsrc_t srdO = mksrd(p.wo[c.br], NH * DH * DM * 2);
  const u32 vo0 = 1024u * (ln >> 3) + 16u * ((ln & 7) ^ (2u * ((ln >> 4) & 1))), vo1 = vo0 ^ 64u;
  const u32 hs = (0x2013u >> (4u * ((u >> 2) & 3))) & 3u;
  const u32 so = (u < 16) ? ((c.hg * 4 + hs) * 128u + 32u * (u & 3)) * 1024u + 128u * c.wave : 0x01000000u;
  const u32 lb = c.ldsb + ring_slot(c.wave, k);
  dma_wo(rfl(lb), vo0, srdO, rfl(so));                     dma_wo(rfl(lb + 1024u), vo1, srdO, rfl(so + 8192u));
  dma_wo(rfl(lb + 2048u), vo0, srdO, rfl(so + 16384u));     dma_wo(rfl(lb + 3072u), vo1, srdO, rfl(so + 24576u));
}

DI void issue_kv(Ctx& c, const __amdgpu_buffer_rsrc_t srd, u32 st, u32 la, u32 vo0, u32 vo1, u32 vo2, u32 vo3) {
  asm volatile("s_mov_b32 m0, %0\n\ts_nop 0\n\t"
               "buffer_load_dwordx4 %1, %5, %6 offen" KVPOL " lds\n\t"
               "buffer_load_dwordx4 %2, %5, %6 offen offset:1024" KVPOL " lds\n\t"
               "buffer_load_dwordx4 %3, %5, %6 offen offset:2048" KVPOL " lds\n\t"
               "buffer_load_dwordx4 %4, %5, %6 offen offset:3072" KVPOL " lds"
               :: "s"(rfl(c.ldsb + la + 4096u * c.w)), "v"(vo0), "v"(vo1), "v"(vo2), "v"(vo3), "s"(srd), "s"(rfl(st * SLOT + 4096u * c.w)) : "memory");
}

DI u32 wq_pattern(const Ctx& c, int ln) { return 8192u * (ln >> 3) + 16u * ((ln & 7) ^ (2u * (((ln >> 4) & 1) + 2u * (c.w & 1)))); }
DI void issue_wq(const Ctx& c, const __amdgpu_buffer_rsrc_t srdW, u32 vo_w, u32 rdn, u32 pi, u32 la) {
  const u32 b = c.b, j = c.j; (void)j; (void)b;
  const u32 hd = c.hg * 4 + rdn;
  const u32 kq = ((pi & 3) + KROT) & 3, dh = (pi >> 2) ^ DROT;
  const u32 so = (128u * kq + 8u * c.w) * 8192u + (128u * hd + 64u * dh) * 2u;
  #pragma unroll
  for (int i = 0; i < 4; ++i) dma_w(rfl(c.ldsb + la + 1024u * c.w + 4096u * i), vo_w, srdW, rfl(so + 32u * 8192u * i));
}
DI void issue_x_rows(const Params& p, Ctx& c, int ln) {
  const __amdgpu_buffer_rsrc_t srdX = mksrd((const char*)p.x[c.br] + (size_t)c.b * SQ * DM * 2, SQ * DM * 2);
  const u32 start = 56u * (c.j & 1);
  auto one = [&](u32 row) {
    row += start; row = row >= 112u ? row - 112u : row;
    dma_w(rfl(c.ldsb + 1024u * row), (16u * ln) ^ rfl(16u * (row & 15u)), srdX, rfl(1024u * row));
  };
  if (c.wave < 4) {
    #pragma unroll
    for (int i = 0; i < 16; ++i) one(c.w + 4u * i);
  } else {
    #pragma unroll
    for (int i = 0; i < 12; ++i) one(64u + c.w + 4u * i);
  }
}

template <int NQ> struct Proj {
  bf16x8 xf[NQ][16];
  f32x4 qa[4][NQ];
  u32 wA, rowq, oW;
  DI void init(const Ctx& c, int ln) {
    const u32 cl = ln & 15, g = ln >> 4, q4 = cl >> 2, p4 = ln & 3;
    wA = 128u * (8u * g + q4) + 16u * ((2u * ((q4 >> 1) + 2u * (g & 1))) | (p4 >> 1)) + 8u * (p4 & 1);
    rowq = L_QH + 256u * (32u * c.w + cl);
    oW = 16u * ((g >> 1) ^ cl) + 8u * (g & 1);
  }
  DI void read_x(const Ctx& c, int ln) {
    const u32 b = c.b, j = c.j; (void)j; (void)b;
    const u32 cl = ln & 15, g = ln >> 4;
    const u32 xA = 1024u * (32u * c.w + cl) + 16u * (g ^ cl);
    #pragma unroll
    for (int jq = 0; jq < 4; ++jq) {
      const u32 a = xA + 256u * (((u32)jq + KROT) & 3u);
      #pragma unroll
      for (int i = 0; i < NQ; ++i)
        #pragma unroll
        for (int ks = 0; ks < 4; ++ks) xf[i][4 * jq + ks] = lds_b128(c.L, (a ^ (64u * ks)) + 16384u * i);
    }
  }
  template <int PFV = PF>
  DI void piece(const Ctx& c, const int kq, u32 la, u32 dh) {
    LAS char* L = c.L;
    OPAQUE_S(la);
    u32 wa[4];
    #pragma unroll
    for (int d = 0; d < 4; ++d) wa[d] = la + (wA ^ (32u * d));
    auto wread = [&](const int n) { return lds_tr2(L, wa[n & 3] + 4096u * (n >> 2), wa[n & 3] + 4096u * (n >> 2) + 512u); };
    bf16x8 fr[16];
    #pragma unroll
    for (int n = 0; n < 2 * PFV; ++n) fr[n] = wread(n);
    SB();
    #pragma unroll
    for (int hp = 0; hp < 8; ++hp) {
      const int young = 4 * ((7 - hp) < (PFV - 1) ? (7 - hp) : (PFV - 1));
      if (young >= 12) waitlgkm<12>(); else if (young == 8) waitlgkm<8>(); else if (young == 4) waitlgkm<4>(); else waitlgkm<0>();
      SB();
      #pragma unroll
      for (int e = 0; e < 2; ++e) {
        const int n = 2 * hp + e, ks = n >> 2, d = n & 3;
        #pragma unroll
        for (int i = 0; i < NQ; ++i) { if (kq == 0 && ks == 0) MFMA_ZERO(qa[d][i], fr[n], xf[i][4 * kq + ks]); else MFMA_ACC(qa[d][i], fr[n], xf[i][4 * kq + ks]); }
      }
      SB();
      if (hp + PFV < 8) { fr[2 * (hp + PFV)] = wread(2 * (hp + PFV)); fr[2 * (hp + PFV) + 1] = wread(2 * (hp + PFV) + 1); }
      SB();
    }
    if (kq == 3) {
      NOP8();
      SB();
      #pragma unroll
      for (int i = 0; i < NQ; ++i)
        #pragma unroll
        for (int d = 0; d < 4; ++d) lds_w64(L, rowq + 4096u * i + (oW ^ (32u * (4u * dh + d))), pack4(qa[d][i], QSCALE));
    }
  }
};

template <int NQ>
DI void attention_role(const Params& p, Ctx& c, bf16x8 (&park)[16]) {
  LAS char* L = c.L;
  const u32 w = c.w, wave = c.wave; (void)wave;
  const int ln = lane_id();
  const u32 cl = ln & 15, g = ln >> 4, q4 = cl >> 2, p4 = ln & 3;
  const u32 pi_ = (cl & 3) | ((cl & 4) << 1) | ((cl & 8) >> 1);
  const u32 vk0 = 256u * g + 16u * (cl ^ g), vk1 = vk0 ^ 64u, vk2 = vk0 ^ 128u, vk3 = vk0 ^ 192u;
  const u32 kA = 256u * pi_ + 16u * (g ^ pi_);
  const u32 vA = 256u * (8u * (g & 1) + 4u * (g >> 1) + q4) + 16u * ((2u * (q4 + 4u * (g & 1))) | (p4 >> 1)) + 8u * (p4 & 1);
  const u32 rowq = L_QH + 256u * (32u * w + cl);
  const u32 qA = rowq + 16u * (g ^ cl);
  const u32 oW = 16u * ((g >> 1) ^ cl) + 8u * (g & 1);
  u64 d0 = 0;
  {
    const u32 b = c.b, j = c.j; (void)j; (void)b;
    const __amdgpu_buffer_rsrc_t srdW = mksrd(p.wq[c.br], DM * NH * DH * 2);
    const u32 vo_w = wq_pattern(c, ln);
    const u32 dh1 = DROT ^ 1u;
    Proj<NQ> pj;
    pj.init(c, ln);
    if (NQ == 1) d0 = ld_agent((const char*)p.ws + WS_DONE + 128u * member_bid(c, ln & 7));
    issue_x_rows(p, c, ln); issue_wq(c, srdW, vo_w, 0, 4, 8 * SLOT);
    barrier_vm<4>();
    if (NQ == 1) PIN(d0);
    pj.read_x(c, ln);
    barrier_vm<0>();
    issue_wq(c, srdW, vo_w, 0, 5, 3 * SLOT); issue_wq(c, srdW, vo_w, 0, 6, 4 * SLOT);
    pj.template piece<PFP>(c, 0, 8 * SLOT, dh1);
    barrier_vm<4>();
    issue_wq(c, srdW, vo_w, 0, 7, 2 * SLOT);
    pj.template piece<PFP>(c, 1, 3 * SLOT, dh1);
    issue_kv(c, c.srdK, 0, L_K, vk0, vk1, vk2, vk3);
    barrier_vm<8>();
    pj.template piece<PFP>(c, 2, 4 * SLOT, dh1);
    issue_kv(c, c.srdK, 1, L_K + SLOT, vk0, vk1, vk2, vk3);
    barrier_vm<8>();
    pj.template piece<PFP>(c, 3, 2 * SLOT, dh1);
    barrier_vm<4>();
  }
  issue_kv(c, c.srdK, 2, L_K + 2 * SLOT, vk0, vk1, vk2, vk3);
  bf16x8 kf[16];
  #pragma unroll
  for (int i = 0; i < 16; ++i) kf[i] = lds_b128(L, L_K + (kA ^ (64u * (i & 3))) + 4096u * (i >> 2));
  barrier_vm<4>();
  STAMP(1); STAMPS(0);
  bf16x8 qf[NQ][4];
  if (NQ == 1) {
    #pragma unroll
    for (int i = 0; i < 16; ++i) park[i] = bf16x8{};
  }
  bf16x8 ones = __builtin_bit_cast(bf16x8, u32x4{0x3F803F80u, 0x3F803F80u, 0x3F803F80u, 0x3F803F80u});
  PIN(ones);
  u32 s3 = 0;
  f32x4 O[8][NQ], negm[NQ], l[NQ];
  float m[NQ];
  #pragma unroll
  for (int i = 0; i < NQ; ++i) {
    m[i] = 0.f; negm[i] = f32x4{}; l[i] = f32x4{1.f, 1.f, 1.f, 1.f};
    #pragma unroll
    for (int d = 0; d < 8; ++d) O[d][i] = f32x4{};
  }
  TICKDECL
  auto step = [&](auto first_tag, const int rd, const int t) {
    constexpr bool FIRST = decltype(first_tag)::value;
    const u32 s3n = (s3 == 2) ? 0 : s3 + 1;
    const u32 vbase = L_V + s3 * SLOT, kbase = L_K + s3n * SLOT;
    const u32 sK = 8u * rd + t + 3;
    const bool kdma = FIRST || sK < 32;
    const u32 kdst = L_K + s3 * SLOT;
    TICK(0);
    float inv[NQ];
    if (FIRST) {
      #pragma unroll
      for (int i = 0; i < NQ; ++i)
        #pragma unroll
        for (int ks = 0; ks < 4; ++ks) qf[i][ks] = lds_b128(L, (qA ^ (64u * ks)) + 4096u * i);
      #pragma unroll
      for (int i = 0; i < NQ; ++i) inv[i] = __builtin_amdgcn_rcpf(l[i][0]);
      if (NQ == 1 && rd == 3) {
        if (ln < 8) { *lp<u64>(L_FLAG + 8u * ln) = d0; st_agent((char*)p.ws + WS_ACK + 128u * c.bid + 8u * ln, d0 ^ ACK_MIX); }
      }
    }
    if (KPOS == 0 && kdma) issue_kv(c, c.srdK, sK, kdst, vk0, vk1, vk2, vk3);
    if (!FIRST && sK == 34) { const int l2 = lane_id(); issue_wo(p, c, l2, 0, 0); issue_wo(p, c, l2, 1, 1); }
    TICK(1);
    if (!FIRST && NQ == 1 && rd == 1 && t == 1) {
      #pragma unroll
      for (int i = 0; i < 16; ++i) park[i] = lds_b128(L, ((L_QH + 256u * cl + 16u * (g ^ cl)) ^ (64u * (i & 3))) + 4096u * ((i >> 2) == 3 ? 6 : (i >> 2)));
    }
    u32 va[8];
    va[0] = vbase + vA;
    #pragma unroll
    for (int d = 1; d < 8; ++d) va[d] = va[0] ^ (32u * d);
    auto vread = [&](const int d, const int u) { return lds_tr2(L, va[d] + 8192u * u, va[d] + 8192u * u + 4096u); };
    if (FIRST) { waitlgkm<0>(); SB(); }
    f32x4 S[4][NQ];
    bf16x8 vf[16];
    #pragma unroll
    for (int kt = 0; kt < 2; ++kt)
      #pragma unroll
      for (int ks = 0; ks < 4; ++ks) {
        #pragma unroll
        for (int i = 0; i < NQ; ++i) { if (ks == 0) MFMA_INIT(S[kt][i], kf[4 * kt + ks], qf[i][ks], negm[i]); else MFMA_ACC(S[kt][i], kf[4 * kt + ks], qf[i][ks]); }
        vf[4 * kt + ks] = vread(4 * kt + ks, 0);
        if (FIRST) {
          const int d = 4 * kt + ks;
          #pragma unroll
          for (int i = 0; i < NQ; ++i) lds_w64(L, rowq + 4096u * i + (oW ^ (32u * d)), pack4(O[d][i], inv[i]));
        }
        SB();
      }
    u32x4 pw[NQ][2];
    float pa[NQ];
    #pragma unroll
    for (int kt = 2; kt < 4; ++kt)
      #pragma unroll
      for (int ks = 0; ks < 4; ++ks) {
        const int n = 4 * (kt - 2) + ks;
        #pragma unroll
        for (int i = 0; i < NQ; ++i) { if (ks == 0) MFMA_INIT(S[kt][i], kf[4 * kt + ks], qf[i][ks], negm[i]); else MFMA_ACC(S[kt][i], kf[4 * kt + ks], qf[i][ks]); }
        vf[8 + n] = vread(n, 1);
        if (n >= 2 && n < 6) {
          const int k2 = (n - 2) >> 1, e = 2 * ((n - 2) & 1);
          #pragma unroll
          for (int i = 0; i < NQ; ++i) {
            const float x0 = S[k2][i][e], x1 = S[k2][i][e + 1];
            pa[i] = (n == 2) ? __builtin_elementwise_maximum(x0, x1) : vmax3(pa[i], x0, x1);
            u32 pk = cvtpk(EXP2(x0), EXP2(x1)); PIN(pk);
            pw[i][0][2 * k2 + (e >> 1)] = pk;
          }
        }
        SB();
      }
    asm volatile("" ::: "memory");
    NOP8();
    SB();
    TICK(2);
    #pragma unroll
    for (int i = 0; i < NQ; ++i) {
      const float pb = vmax3(S[2][i][0], S[2][i][1], S[2][i][2]);
      pa[i] = vmax3(pa[i], S[2][i][3], S[3][i][0]); pa[i] = vmax3(pa[i], S[3][i][1], S[3][i][2]);
      pa[i] = vmax3(pa[i], pb, S[3][i][3]);
    }
    bool move;
    if (FIRST) {
      move = false;
      #pragma unroll
      for (int i = 0; i < NQ; ++i) { pa[i] = xg_max(pa[i]); move |= !(__builtin_fabsf(pa[i]) <= THR); }
    } else move = (NQ == 2 ? __builtin_elementwise_maximum(pa[0], pa[NQ - 1]) : pa[0]) > THR;
    if (__builtin_expect(__any(move), 0)) {
      #pragma unroll
      for (int i = 0; i < NQ; ++i) {
        const float delta = FIRST ? pa[i] : fmaxf(xg_max(pa[i]), 0.f);
        m[i] += delta;
        negm[i] = f32x4{-m[i], -m[i], -m[i], -m[i]};
        #pragma unroll
        for (int kt = 0; kt < 4; ++kt) S[kt][i] -= delta;
        #pragma unroll
        for (int k2 = 0; k2 < 2; ++k2)
          #pragma unroll
          for (int e = 0; e < 4; e += 2) pw[i][0][2 * k2 + (e >> 1)] = cvtpk(EXP2(S[k2][i][e]), EXP2(S[k2][i][e + 1]));
        if (!FIRST) {
          const float alpha = EXP2(-delta);
          l[i] *= alpha;
          #pragma unroll
          for (int d = 0; d < 8; ++d) O[d][i] *= alpha;
        }
      }
      SB();
      NOP2();
    }
    if (KPOS == 1 && kdma) issue_kv(c, c.srdK, sK, kdst, vk0, vk1, vk2, vk3);
    waitlgkm<15>();
    SB();
    TICK(3);
    #pragma unroll
    for (int i = 0; i < NQ; ++i) { if (FIRST) MFMA_ZERO(l[i], ones, __builtin_bit_cast(bf16x8, pw[i][0])); else MFMA_ACC(l[i], ones, __builtin_bit_cast(bf16x8, pw[i][0])); }
    #pragma unroll
    for (int d = 0; d < 8; ++d) {
      #pragma unroll
      for (int i = 0; i < NQ; ++i) { if (FIRST) MFMA_ZERO(O[d][i], vf[d], __builtin_bit_cast(bf16x8, pw[i][0])); else MFMA_ACC(O[d][i], vf[d], __builtin_bit_cast(bf16x8, pw[i][0])); }
      if (d < 4) {
        const int k2 = 2 + (d >> 1), e = 2 * (d & 1);
        #pragma unroll
        for (int i = 0; i < NQ; ++i) { u32 pk = cvtpk(EXP2(S[k2][i][e]), EXP2(S[k2][i][e + 1])); PIN(pk); pw[i][1][2 * (k2 - 2) + (e >> 1)] = pk; }
      }
      SB();
    }
    u32 ka[4];
    ka[0] = kbase + kA;
    #pragma unroll
    for (int ks = 1; ks < 4; ++ks) ka[ks] = ka[0] ^ (64u * ks);
    waitlgkm<0>();
    SB();
    #pragma unroll
    for (int i = 0; i < NQ; ++i) MFMA_ACC(l[i], ones, __builtin_bit_cast(bf16x8, pw[i][1]));
    #pragma unroll
    for (int d = 0; d < 8; ++d) {
      #pragma unroll
      for (int i = 0; i < NQ; ++i) MFMA_ACC(O[d][i], vf[8 + d], __builtin_bit_cast(bf16x8, pw[i][1]));
      SB();
      kf[2 * d] = lds_b128(L, ka[(2 * d) & 3] + 4096u * ((2 * d) >> 2));
      kf[2 * d + 1] = lds_b128(L, ka[(2 * d + 1) & 3] + 4096u * ((2 * d + 1) >> 2));
      SB();
    }
    s3 = s3n;
    TICK(4);
    if (FIRST && NQ == 1 && rd == 3) barrier_vm<5>();
    else if (kdma) barrier_vm<4>(); else barrier_vm<0>();
    TICK(5);
    STAMPS(8 * rd + t + 1);
  };
  #pragma unroll 1
  for (int rd = 0; rd < 4; ++rd) {
    step(std::true_type{}, rd, 0);
    #pragma unroll 1
    for (int t = 1; t < 8; ++t) step(std::false_type{}, rd, t);
    STAMP(2 + rd);
  }
  NOP8();
  SB();
  #pragma unroll
  for (int i = 0; i < NQ; ++i) {
    const float inv = __builtin_amdgcn_rcpf(l[i][0]);
    #pragma unroll
    for (int d = 0; d < 8; ++d) lds_w64(L, rowq - L_QH + 4096u * i + (oW ^ (32u * d)), pack4(O[d][i], inv));
  }
  TICKOUT
}

template <int NQ>
DI void projection_role(const Params& p, Ctx& c, bf16x8 (&park)[16]) {
  LAS char* L = c.L;
  const u32 w = c.w, wave = c.wave; (void)wave;
  const u32 b = c.b, j = c.j; (void)j;
  const int ln = lane_id();
  const u32 cl = ln & 15, g = ln >> 4, q4 = cl >> 2, p4 = ln & 3;
  const u32 vv0 = 256u * g + 16u * (cl ^ (2u * g)), vv2 = vv0 ^ 128u;
  const __amdgpu_buffer_rsrc_t srdW = mksrd(p.wq[c.br], DM * NH * DH * 2);
  const u32 rot = KROT, dh0 = DROT;
  const u32 vo_w = wq_pattern(c, ln);
  auto issue_w = [&](u32 rdn, u32 pi, u32 la) { issue_wq(c, srdW, vo_w, rdn, pi, la); };
  Proj<NQ> pj;
  pj.init(c, ln);
  const u32 rowq = pj.rowq;
  const u32 qA = rowq + 16u * (g ^ cl);
  STAMP(13);
  issue_w(0, 0, 7 * SLOT); issue_x_rows(p, c, ln); issue_w(0, 1, 9 * SLOT);
  STAMP(14);
  barrier_vm<4>();
  STAMP(1);
  pj.read_x(c, ln);
  barrier_lgkm();
  issue_w(0, 2, 5 * SLOT);
  pj.template piece<PFP>(c, 0, 7 * SLOT, dh0);
  barrier_vm<4>();
  STAMP(2);
  issue_w(0, 3, 6 * SLOT);
  pj.template piece<PFP>(c, 1, 9 * SLOT, dh0);
  barrier_vm<4>();
  issue_w(1, 0, 7 * SLOT);
  pj.template piece<PFP>(c, 2, 5 * SLOT, dh0);
  issue_kv(c, c.srdV, 0, L_V, vv0, vv0, vv2, vv2);
  barrier_vm<8>();
  STAMP(3);
  issue_kv(c, c.srdV, 1, L_V + SLOT, vv0, vv0, vv2, vv2);
  pj.template piece<PFP>(c, 3, 6 * SLOT, dh0);
  barrier_lgkm();
  STAMP(4);
  barrier_vm<4>();
  STAMP(5);
  bf16x8 og1[NQ][4];
  #pragma unroll
  for (int i = 0; i < NQ; ++i)
    #pragma unroll
    for (int ks = 0; ks < 4; ++ks) og1[i][ks] = bf16x8{};
  if (NQ == 1) {
    #pragma unroll
    for (int i = 4; i < 16; ++i) park[i] = bf16x8{};
  }
  u32 s3 = 0;
  TICKDECL
  #pragma unroll 1
  for (int hh = 0; hh < 6; ++hh) {
    #pragma unroll
    for (int kq = 0; kq < 4; ++kq) {
      const u32 s = 4u * hh + kq, s1 = s + 1;
      const u32 vdst = L_V + ((s3 == 0) ? 2 : s3 - 1) * SLOT;
      TICK(0);
      issue_w((1 + (s1 >> 3)) & 3, s1 & 7, L_W + (kq & 1) * SLOT);
      TICK(1);
      if (NQ == 1 && kq == 1 && hh == 2) {
        #pragma unroll
        for (int i = 4; i < 16; ++i) park[i] = lds_b128(L, ((L_QH + 256u * cl + 16u * (g ^ cl)) ^ (64u * (i & 3))) + 4096u * (2 + (i >> 2)));
      }
      if (kq == 1 && hh == 4) {
        #pragma unroll
        for (int i = 0; i < NQ; ++i)
          #pragma unroll
          for (int ks = 0; ks < 4; ++ks) og1[i][ks] = lds_b128(L, (qA ^ (64u * ks)) + 4096u * i);
      }
      pj.piece(c, kq, L_W + ((kq + 1) & 1) * SLOT, (u32)(hh & 1) ^ dh0);
      issue_kv(c, c.srdV, s + 2, vdst, vv0, vv0, vv2, vv2);
      s3 = (s3 == 2) ? 0 : s3 + 1;
      TICK(2);
      barrier_vm<4>();
      TICK(4);
    }
  }
  #pragma unroll
  for (int i = 0; i < 4 * NQ; ++i) park[i] = bf16x8{};
  const __amdgpu_buffer_rsrc_t srdO = mksrd(p.wo[c.br], NH * DH * DM * 2);
  #pragma unroll 1
  for (u32 s = 24; s < 32; ++s) {
    const u32 vdst = L_V + ((s3 == 0) ? 2 : s3 - 1) * SLOT;
    if (s + 2 < 32) issue_kv(c, c.srdV, s + 2, vdst, vv0, vv0, vv2, vv2);
    if (s == WARMS) {
      #pragma unroll
      for (int i = 0; i < 8; ++i) dma_w(rfl(c.ldsb + L_W + 1024u * w + 4096u * i), 16u * ln, srdO, rfl((c.hg * 512u + 32u * b + 4u * i + w) * 1024u));
    }
    if (s == 31) { issue_wo(p, c, ln, 0, 0); issue_wo(p, c, ln, 1, 1); }
    if (s == 25) {
      #pragma unroll
      for (int i = 0; i < NQ; ++i)
        #pragma unroll
        for (int ks = 0; ks < 4; ++ks) park[4 * i + ks] = lds_b128(L, (qA ^ (64u * ks)) + 4096u * i);
    }
    s3 = (s3 == 2) ? 0 : s3 + 1;
    if (s >= 30) barrier_vm<0>(); else if (s == WARMS || s == WARMS + 1) barrier_vm<12>(); else barrier_vm<4>();
  }
  STAMP(6);
  TICKOUT
  #pragma unroll
  for (int i = 0; i < NQ; ++i)
    #pragma unroll
    for (int ks = 0; ks < 4; ++ks) lds_w128(L, OREG + ((qA - L_QH) ^ (64u * ks)) + 4096u * i, og1[i][ks]);
}

__global__ __launch_bounds__(512, 2) void fused_attn(Params p) {
  extern __shared__ __attribute__((aligned(16))) char smem[];
  Ctx c;
  c.L = (LAS char*)smem;
  c.ldsb = (u32)(size_t)smem;
  c.wave = rfl(threadIdx.x >> 6);
  c.w = c.wave & 3;
  const u32 wave = c.wave, w = c.w;
  const u32 bid = blockIdx.x, xcd = bid & 7;
  c.j = bid >> 3; c.bid = bid;
  c.br = xcd & 1; c.hg = 2 * (xcd >> 1) + (c.j & 1); c.b = c.j >> 1;
  const size_t kvoff = (size_t)((c.b * NH + c.hg * 4) * SK) * DH * 2;
  c.srdK = mksrd((const char*)p.k[c.br] + kvoff, 4 * SK * DH * 2);
  c.srdV = mksrd((const char*)p.v[c.br] + kvoff, 4 * SK * DH * 2);
  STAMP(0); STAMPC(8);
  bf16x8 park[16];
  if (wave < 3) attention_role<2>(p, c, park);
  else if (wave == 3) attention_role<1>(p, c, park);
  else if (wave < 7) projection_role<2>(p, c, park);
  else projection_role<1>(p, c, park);

  {
    LAS char* L = c.L;
    const int ln = lane_id();
    const u32 cl = ln & 15, g = ln >> 4, q4 = cl >> 2, p4 = ln & 3;
    const char* ackp = (const char*)p.ws + WS_ACK + 128u * member_bid(c, ln & 7) + 8u * c.hg;
    u64 ack;
    asm volatile("global_load_dwordx2 %0, %1, off sc1" : "=&v"(ack) : "v"(ackp) : "memory");
    issue_wo(p, c, ln, 2, 2);
    barrier_lgkm();
    STAMP(7);
    const u32 oA = 256u * cl + 16u * (g ^ cl);
    const u32 wA = 128u * (8u * g + q4) + 16u * ((2u * ((q4 >> 1) + 2u * (g & 1))) | (p4 >> 1)) + 8u * (p4 & 1);
    auto aread = [&](bf16x8 (&af)[4], u32 slot3) {
      u32 rb = rfl(ring_slot(wave, slot3));
      OPAQUE_S(rb);
      const u32 a0 = rb + wA;
      #pragma unroll
      for (int nt = 0; nt < 4; ++nt) { const u32 a = a0 ^ (32u * nt); af[nt] = lds_tr2(L, a, a + 512u); }
    };
    f32x4 acc[4][7];
    bf16x8 afA[4], afB[4], bq[7];
    auto oaddr = [&](u32 u) { return OREG * ((u >> 2) & 1) + (oA ^ (64u * (u & 3))); };
    SB();
    aread(afA, 0);
    {
      const u32 ob = oaddr(0);
      #pragma unroll
      for (int qt = 0; qt < 4; ++qt) bq[qt] = lds_b128(L, ob + 4096u * qt);
    }
    u32 sl = 0;
    auto kstep = [&](const u32 u, bf16x8 (&af)[4], bf16x8 (&afn)[4]) {
      const u32 sl1 = (sl == 2) ? 0 : sl + 1;
      const u32 ob = oaddr(u), obn = oaddr(u + 1);
      const bool pre = (u & 7) != 7;
      #pragma unroll
      for (int qt = 0; qt < 7; ++qt) {
        #pragma unroll
        for (int nt = 0; nt < 4; ++nt) { if (u == 0) MFMA_ZERO(acc[nt][qt], af[nt], bq[qt]); else MFMA_ACC(acc[nt][qt], af[nt], bq[qt]); }
        SB();
        if (qt < 3) bq[qt + 4] = lds_b128(L, ob + 4096u * (qt + 4));
        else if (pre) bq[qt - 3] = lds_b128(L, obn + 4096u * (qt - 3));
        if (qt == 0) issue_wo(p, c, ln, u + 3, sl);
        if (qt == 2) { waitvm<8>(); if (u == 0) asm volatile("" : "+v"(ack)); aread(afn, sl1); }
        SB();
      }
      sl = sl1;
    };
    #pragma unroll
    for (u32 u = 0; u < 16; u += 2) {
      if (u == 8) {
        barrier_lgkm();
        if (wave == 3 || wave == 7) {
          #pragma unroll
          for (int i = 0; i < 16; ++i) {
            if (wave == 3) lds_w128(L, (oA ^ (64u * (i & 3))) + 4096u * ((i >> 2) == 3 ? 6 : (i >> 2)), park[i]);
            else if (i >= 4) lds_w128(L, (oA ^ (64u * (i & 3))) + 4096u * (2 + (i >> 2)), park[i]);
          }
        }
        if (wave >= 4) {
          #pragma unroll
          for (int ks = 0; ks < 4; ++ks) {
            lds_w128(L, OREG + (oA ^ (64u * ks)) + 8192u * w, park[ks]);
            if (w < 3) lds_w128(L, OREG + (oA ^ (64u * ks)) + 8192u * w + 4096u, park[4 + ks]);
          }
        }
        barrier_lgkm();
        const u32 ob = oaddr(8);
        #pragma unroll
        for (int qt = 0; qt < 4; ++qt) bq[qt] = lds_b128(L, ob + 4096u * qt);
      }
      kstep(u, afA, afB);
      kstep(u + 1, afB, afA);
    }
    waitvm<0>();
    STAMP(11);
    NOP8();
    SB();
    const __amdgpu_buffer_rsrc_t srdP = mksrd((const char*)p.ws + WS_P + (size_t)(c.br * NB + c.b) * 8 * PART_WG, 8 * PART_WG);
    #pragma unroll
    for (int qt = 0; qt < 7; ++qt)
      #pragma unroll
      for (int np = 0; np < 2; ++np) {
        const u32x2 a = pack4n(acc[2 * np][qt]), bb = pack4n(acc[2 * np + 1][qt]);
        __builtin_amdgcn_raw_buffer_store_b128(u32x4{a[0], a[1], bb[0], bb[1]}, srdP, c.hg * PART_WG + wave * 14336u + (qt * 2 + np) * 1024u + ln * 16u, 0, 16);
      }
    waitvm<0>();
    barrier_lgkm();
    STAMP(11);
    if (wave == 0) {
      const u64 e0 = *lp<u64>(L_FLAG + 8u * (ln & 7));
      const u64 want = *lp<u64>(L_FLAG + 8u * c.hg) ^ ACK_MIX;
      for (u32 spin = 0; !__all(ack == want) && spin < SPIN_CAP; ++spin) { __builtin_amdgcn_s_sleep(8); ack = ld_agent(ackp); }
      if (ln == (int)c.hg) st_agent((char*)p.ws + WS_DONE + 128u * c.bid, e0 + 1);
      const char* dp = (const char*)p.ws + WS_DONE + 128u * member_bid(c, ln & 7);
      for (u32 spin = 0; spin < SPIN_CAP; ++spin) { if (__all(ld_agent(dp) == e0 + 1)) break; __builtin_amdgcn_s_sleep(2); }
    }
    barrier_lgkm();
    STAMP(12);
    if (wave < 7) {
      u32x4 x[2][8];
      #pragma unroll
      for (int np = 0; np < 2; ++np)
        #pragma unroll
        for (int m = 0; m < 8; ++m) x[np][m] = __builtin_amdgcn_raw_buffer_load_b128(srdP, m * PART_WG + c.hg * 14336u + (wave * 2 + np) * 1024u + ln * 16u, 0, 16);
      const u32 tile = ring_slot(wave, 0);
      #pragma unroll
      for (int np = 0; np < 2; ++np) {
        float s8[8] = {};
        #pragma unroll
        for (int m = 0; m < 8; ++m)
          #pragma unroll
          for (int i = 0; i < 4; ++i) { s8[2 * i] += __uint_as_float(x[np][m][i] << 16); s8[2 * i + 1] += __uint_as_float(x[np][m][i] & 0xffff0000u); }
        lds_w64(L, tile + cl * 128u + 64u * np + 8u * g, u32x2{cvtpk(s8[0], s8[1]), cvtpk(s8[2], s8[3])});
        lds_w64(L, tile + cl * 128u + 64u * np + 32u + 8u * g, u32x2{cvtpk(s8[4], s8[5]), cvtpk(s8[6], s8[7])});
      }
      #pragma unroll
      for (int i = 0; i < 2; ++i) {
        const u32 row = 8u * i + (ln >> 3), ch = ln & 7;
        char* o = (char*)p.out + (((size_t)c.b * SQ + 16u * wave + row) * (2 * DM) + c.br * DM + 64u * c.hg) * 2 + 16u * ch;
        __builtin_nontemporal_store(lds_b128(L, tile + row * 128u + 16u * ch), (bf16x8*)o);
      }
    }
  }
  STAMP(10); STAMPC(9);
  STAMPOUT();
}

extern "C" void kernel_launch(void* const* d_in, const int* in_sizes, int n_in, void* d_out, int out_size, void* d_ws, size_t ws_size,
                              hipStream_t stream) {
  Params p;
  for (int i = 0; i < 2; ++i) { p.x[i] = d_in[5 * i]; p.k[i] = d_in[5 * i + 1]; p.v[i] = d_in[5 * i + 2]; p.wq[i] = d_in[5 * i + 3]; p.wo[i] = d_in[5 * i + 4]; }
  p.out = d_out; p.ws = d_ws;
  (void)hipFuncSetAttribute((const void*)fused_attn, hipFuncAttributeMaxDynamicSharedMemorySize, LDS_BYTES);
  fused_attn<<<256, 512, LDS_BYTES, stream>>>(p);
}
```

```cpp
#include <hip/hip_runtime.h>
#include <hip/hip_bf16.h>
#include <type_traits>

typedef unsigned u32;
typedef unsigned long long u64;
typedef __bf16 bf16x8 __attribute__((ext_vector_type(8)));
typedef __bf16 bf16x4 __attribute__((ext_vector_type(4)));
typedef __bf16 bf16x2 __attribute__((ext_vector_type(2)));
typedef float f32x4 __attribute__((ext_vector_type(4)));
typedef float f32x2 __attribute__((ext_vector_type(2)));
typedef u32 u32x4 __attribute__((ext_vector_type(4)));
typedef u32 u32x2 __attribute__((ext_vector_type(2)));
#define LAS __attribute__((address_space(3)))
#define DI __device__ __forceinline__

constexpr int NB = 16, SQ = 112, DM = 512, NH = 32, SK = 512, DH = 128;
constexpr float QSCALE = 0.12751743082f;
constexpr float THR = 8.0f;

constexpr u32 SLOT = 16384;
constexpr u32 L_K = 0;
constexpr u32 L_V = 3 * SLOT;
constexpr u32 L_W = 6 * SLOT;
constexpr u32 L_QH = 8 * SLOT;
constexpr u32 LDS_BYTES = 10 * SLOT;
constexpr u32 OREG = 28672;
constexpr u32 PIECE = 4096;
constexpr u32 L_RS0 = 90112, L_RS1 = 122880, L_RS2 = 2 * OREG;

constexpr size_t WS_P = 0;
constexpr u32 PART_WG = 8 * 7 * 2 * 1024;
constexpr size_t WS_DONE = 32u << 20, WS_ACK = WS_DONE + 65536;
constexpr u32 L_FLAG = 159744;
constexpr unsigned long long ACK_MIX = 0x9E3779B97F4A7C15ull;
constexpr u32 SPIN_CAP = 1u << 18;

struct Params {
  const void* x[2]; const void* k[2]; const void* v[2]; const void* wq[2]; const void* wo[2];
  void* out; void* ws;
};

DI u32 rfl(u32 x) { return __builtin_amdgcn_readfirstlane(x); }
DI __amdgpu_buffer_rsrc_t mksrd(const void* p, u32 bytes) { return __builtin_amdgcn_make_buffer_rsrc((void*)p, (short)0, (int)bytes, 0x00020000); }
DI void dma_nt(u32 m0v, u32 voff, __amdgpu_buffer_rsrc_t srd, u32 soff) {
  asm volatile("s_mov_b32 m0, %0\n\ts_nop 0\n\tbuffer_load_dwordx4 %1, %2, %3 offen nt lds" :: "s"(m0v), "v"(voff), "s"(srd), "s"(soff) : "memory");
}
#define KVPOL " nt"
#define WPOL0 ""
#define WPOL1 ""
DI void dma_wo(u32 m0v, u32 voff, __amdgpu_buffer_rsrc_t srd, u32 soff) {
  asm volatile("s_mov_b32 m0, %0\n\ts_nop 0\n\tbuffer_load_dwordx4 %1, %2, %3 offen" WPOL1 " lds" :: "s"(m0v), "v"(voff), "s"(srd), "s"(soff) : "memory");
}
DI void dma_w(u32 m0v, u32 voff, __amdgpu_buffer_rsrc_t srd, u32 soff) {
  asm volatile("s_mov_b32 m0, %0\n\ts_nop 0\n\tbuffer_load_dwordx4 %1, %2, %3 offen" WPOL0 " lds" :: "s"(m0v), "v"(voff), "s"(srd), "s"(soff) : "memory");
}
#define KROTM 1
#define PF 3
#define PFP 6
#define WARMS 29
#define VEARLY2 1
#define WSPLIT 1
#define KPOS 1
#define KROT (KROTM == 1 ? (((b >> 2) + 2 * (j & 1)) & 3) : 0)
#define DROT (KROTM == 1 ? ((b >> 1) & 1) : 0)

template <int N> DI void waitvm() { asm volatile("s_waitcnt vmcnt(%0)" :: "i"(N) : "memory"); }
template <int N> DI void waitlgkm() { __builtin_amdgcn_s_waitcnt(0xC07F | (N << 8)); }
DI void barrier_lgkm() { asm volatile("" ::: "memory"); waitlgkm<0>(); __builtin_amdgcn_s_barrier(); asm volatile("" ::: "memory"); }
template <int N> DI void barrier_vm() { asm volatile("s_waitcnt vmcnt(%0)" :: "i"(N) : "memory"); waitlgkm<0>(); __builtin_amdgcn_s_barrier(); asm volatile("" ::: "memory"); }
DI u32 cvtpk(float lo, float hi) { f32x2 v = {lo, hi}; bf16x2 b = __builtin_convertvector(v, bf16x2); return __builtin_bit_cast(u32, b); }
DI u32x2 pack4(const f32x4& a, float s) { return u32x2{cvtpk(a[0] * s, a[1] * s), cvtpk(a[2] * s, a[3] * s)}; }
DI u32x2 pack4n(const f32x4& a) { return u32x2{cvtpk(a[0], a[1]), cvtpk(a[2], a[3])}; }
template <class T> DI LAS T* lp(u32 off) { return (LAS T*)off; }
DI bf16x8 lds_b128(LAS char*, u32 off) { return *lp<bf16x8>(off); }
DI void lds_w128(LAS char*, u32 off, bf16x8 v) { *lp<bf16x8>(off) = v; }
DI void lds_w64(LAS char*, u32 off, u32x2 v) { *lp<u32x2>(off) = v; }
DI bf16x8 lds_tr2(LAS char*, u32 off0, u32 off1) {
  bf16x4 a = __builtin_amdgcn_ds_read_tr16_b64_v4bf16(lp<bf16x4>(off0));
  bf16x4 b = __builtin_amdgcn_ds_read_tr16_b64_v4bf16(lp<bf16x4>(off1));
  return __builtin_shufflevector(a, b, 0, 1, 2, 3, 4, 5, 6, 7);
}
#define STAMP(i)
#define STAMPC(i)
#define STAMPOUT()
#define STAMPS(i)
#define SB() __builtin_amdgcn_sched_barrier(0)
#define TICK(i)
#define TICKDECL
#define TICKOUT
#define PIN(x) asm volatile("" : "+v"(x))
#define OPAQUE_S(x) asm volatile("" : "+s"(x))
DI float vmax3(float a, float b, float c) { return __builtin_elementwise_maximum(__builtin_elementwise_maximum(a, b), c); }
#define MFMA_ACC(acc, a, b) asm volatile("v_mfma_f32_16x16x32_bf16 %0, %1, %2, %0" : "+v"(acc) : "v"(a), "v"(b))
#define MFMA_INIT(acc, a, b, c) asm volatile("v_mfma_f32_16x16x32_bf16 %0, %1, %2, %3" : "=&v"(acc) : "v"(a), "v"(b), "v"(c))
#define MFMA_ZERO(acc, a, b) asm volatile("v_mfma_f32_16x16x32_bf16 %0, %1, %2, 0" : "=&v"(acc) : "v"(a), "v"(b))
#define NOP8() asm volatile("s_nop 7")
#define NOP2() asm volatile("s_nop 1")
#define EXP2(x) __builtin_amdgcn_exp2f(x)
#define GAS __attribute__((address_space(1)))
DI u64 ld_agent(const char* p) { return __hip_atomic_load((const GAS u64*)p, __ATOMIC_RELAXED, __HIP_MEMORY_SCOPE_AGENT); }
DI void st_agent(char* p, u64 v) { __hip_atomic_store((GAS u64*)p, v, __ATOMIC_RELAXED, __HIP_MEMORY_SCOPE_AGENT); }
DI int lane_id() { int ln; asm volatile("v_mbcnt_lo_u32_b32 %0, -1, 0\n\tv_mbcnt_hi_u32_b32 %0, -1, %0" : "=v"(ln)); return ln; }
DI float xg_max(float x) {
  auto a = __builtin_amdgcn_permlane16_swap(__float_as_uint(x), __float_as_uint(x), false, false);
  x = fmaxf(__uint_as_float(a[0]), __uint_as_float(a[1]));
  auto b = __builtin_amdgcn_permlane32_swap(__float_as_uint(x), __float_as_uint(x), false, false);
  return fmaxf(__uint_as_float(b[0]), __uint_as_float(b[1]));
}
DI float xg_sum(float x) {
  auto a = __builtin_amdgcn_permlane16_swap(__float_as_uint(x), __float_as_uint(x), false, false);
  x = __uint_as_float(a[0]) + __uint_as_float(a[1]);
  auto b = __builtin_amdgcn_permlane32_swap(__float_as_uint(x), __float_as_uint(x), false, false);
  return __uint_as_float(b[0]) + __uint_as_float(b[1]);
}

struct Ctx {
  LAS char* L; u32 ldsb, wave, w, br, b, hg, j, bid;
  __amdgpu_buffer_rsrc_t srdK, srdV;
};

DI u32 member_bid(const Ctx& c, u32 m) { return 8u * (2u * c.b + (m & 1u)) + c.br + 2u * (m >> 1); }

DI u32 ring_slot(u32 wave, u32 k) { return (k == 0 ? L_RS0 : k == 1 ? L_RS1 : L_RS2) + wave * PIECE; }
DI void issue_wo(const Params& p, const Ctx& c, int ln, u32 u, u32 k) {
  const __amdgpu_buffer_rsrc_t srdO = mksrd(p.wo[c.br], NH * DH * DM * 2);
  const u32 vo0 = 1024u * (ln >> 3) + 16u * ((ln & 7) ^ (2u * ((ln >> 4) & 1))), vo1 = vo0 ^ 64u;
  const u32 hs = (0x2013u >> (4u * ((u >> 2) & 3))) & 3u;
  const u32 so = (u < 16) ? ((c.hg * 4 + hs) * 128u + 32u * (u & 3)) * 1024u + 128u * c.wave : 0x01000000u;
  const u32 lb = c.ldsb + ring_slot(c.wave, k);
  dma_wo(rfl(lb), vo0, srdO, rfl(so));                     dma_wo(rfl(lb + 1024u), vo1, srdO, rfl(so + 8192u));
  dma_wo(rfl(lb + 2048u), vo0, srdO, rfl(so + 16384u));     dma_wo(rfl(lb + 3072u), vo1, srdO, rfl(so + 24576u));
}

DI void issue_kv(Ctx& c, const __amdgpu_buffer_rsrc_t srd, u32 st, u32 la, u32 vo0, u32 vo1, u32 vo2, u32 vo3) {
  asm volatile("s_mov_b32 m0, %0\n\ts_nop 0\n\t"
               "buffer_load_dwordx4 %1, %5, %6 offen" KVPOL " lds\n\t"
               "buffer_load_dwordx4 %2, %5, %6 offen offset:1024" KVPOL " lds\n\t"
               "buffer_load_dwordx4 %3, %5, %6 offen offset:2048" KVPOL " lds\n\t"
               "buffer_load_dwordx4 %4, %5, %6 offen offset:3072" KVPOL " lds"
               :: "s"(rfl(c.ldsb + la + 4096u * c.w)), "v"(vo0), "v"(vo1), "v"(vo2), "v"(vo3), "s"(srd), "s"(rfl(st * SLOT + 4096u * c.w)) : "memory");
}

DI u32 wq_pattern(const Ctx& c, int ln) { return 8192u * (ln >> 3) + 16u * ((ln & 7) ^ (2u * (((ln >> 4) & 1) + 2u * (c.w & 1)))); }
DI void issue_wq_quarter(const Ctx& c, const __amdgpu_buffer_rsrc_t srdW, u32 vo_even, u32 rdn, u32 pi, u32 la, const u32 q) {
  const u32 b = c.b, j = c.j; (void)j; (void)b;
  const u32 hd = c.hg * 4 + rdn;
  const u32 kq = ((pi & 3) + KROT) & 3, dh = (pi >> 2) ^ DROT;
  const u32 so = (128u * kq + 8u * q) * 8192u + (128u * hd + 64u * dh) * 2u;
  const u32 vo = vo_even ^ (64u * (q & 1));
  #pragma unroll
  for (int i = 0; i < 4; ++i) dma_w(rfl(c.ldsb + la + 1024u * q + 4096u * i), vo, srdW, rfl(so + 32u * 8192u * i));
}
DI void issue_wq(const Ctx& c, const __amdgpu_buffer_rsrc_t srdW, u32 vo_w, u32 rdn, u32 pi, u32 la) {
  const u32 b = c.b, j = c.j; (void)j; (void)b;
  const u32 hd = c.hg * 4 + rdn;
  const u32 kq = ((pi & 3) + KROT) & 3, dh = (pi >> 2) ^ DROT;
  const u32 so = (128u * kq + 8u * c.w) * 8192u + (128u * hd + 64u * dh) * 2u;
  #pragma unroll
  for (int i = 0; i < 4; ++i) dma_w(rfl(c.ldsb + la + 1024u * c.w + 4096u * i), vo_w, srdW, rfl(so + 32u * 8192u * i));
}
DI void issue_x_rows(const Params& p, Ctx& c, int ln) {
  const __amdgpu_buffer_rsrc_t srdX = mksrd((const char*)p.x[c.br] + (size_t)c.b * SQ * DM * 2, SQ * DM * 2);
  const u32 start = 56u * (c.j & 1);
  auto one = [&](u32 row) {
    row += start; row = row >= 112u ? row - 112u : row;
    dma_w(rfl(c.ldsb + 1024u * row), (16u * ln) ^ rfl(16u * (row & 15u)), srdX, rfl(1024u * row));
  };
  if (c.wave < 4) {
    #pragma unroll
    for (int i = 0; i < 16; ++i) one(c.w + 4u * i);
  } else {
    #pragma unroll
    for (int i = 0; i < 12; ++i) one(64u + c.w + 4u * i);
  }
}

template <int NQ> struct Proj {
  bf16x8 xf[NQ][16];
  f32x4 qa[4][NQ];
  u32 wA, rowq, oW;
  DI void init(const Ctx& c, int ln) {
    const u32 cl = ln & 15, g = ln >> 4, q4 = cl >> 2, p4 = ln & 3;
    wA = 128u * (8u * g + q4) + 16u * ((2u * ((q4 >> 1) + 2u * (g & 1))) | (p4 >> 1)) + 8u * (p4 & 1);
    rowq = L_QH + 256u * (32u * c.w + cl);
    oW = 16u * ((g >> 1) ^ cl) + 8u * (g & 1);
  }
  DI void read_x(const Ctx& c, int ln) {
    const u32 b = c.b, j = c.j; (void)j; (void)b;
    const u32 cl = ln & 15, g = ln >> 4;
    const u32 xA = 1024u * (32u * c.w + cl) + 16u * (g ^ cl);
    #pragma unroll
    for (int jq = 0; jq < 4; ++jq) {
      const u32 a = xA + 256u * (((u32)jq + KROT) & 3u);
      #pragma unroll
      for (int i = 0; i < NQ; ++i)
        #pragma unroll
        for (int ks = 0; ks < 4; ++ks) xf[i][4 * jq + ks] = lds_b128(c.L, (a ^ (64u * ks)) + 16384u * i);
    }
  }
  template <int PFV = PF>
  DI void piece(const Ctx& c, const int kq, u32 la, u32 dh) {
    LAS char* L = c.L;
    OPAQUE_S(la);
    u32 wa[4];
    #pragma unroll
    for (int d = 0; d < 4; ++d) wa[d] = la + (wA ^ (32u * d));
    auto wread = [&](const int n) { return lds_tr2(L, wa[n & 3] + 4096u * (n >> 2), wa[n & 3] + 4096u * (n >> 2) + 512u); };
    bf16x8 fr[16];
    #pragma unroll
    for (int n = 0; n < 2 * PFV; ++n) fr[n] = wread(n);
    SB();
    #pragma unroll
    for (int hp = 0; hp < 8; ++hp) {
      const int young = 4 * ((7 - hp) < (PFV - 1) ? (7 - hp) : (PFV - 1));
      if (young >= 12) waitlgkm<12>(); else if (young == 8) waitlgkm<8>(); else if (young == 4) waitlgkm<4>(); else waitlgkm<0>();
      SB();
      #pragma unroll
      for (int e = 0; e < 2; ++e) {
        const int n = 2 * hp + e, ks = n >> 2, d = n & 3;
        #pragma unroll
        for (int i = 0; i < NQ; ++i) { if (kq == 0 && ks == 0) MFMA_ZERO(qa[d][i], fr[n], xf[i][4 * kq + ks]); else MFMA_ACC(qa[d][i], fr[n], xf[i][4 * kq + ks]); }
      }
      SB();
      if (hp + PFV < 8) { fr[2 * (hp + PFV)] = wread(2 * (hp + PFV)); fr[2 * (hp + PFV) + 1] = wread(2 * (hp + PFV) + 1); }
      SB();
    }
    if (kq == 3) {
      NOP8();
      SB();
      #pragma unroll
      for (int i = 0; i < NQ; ++i)
        #pragma unroll
        for (int d = 0; d < 4; ++d) lds_w64(L, rowq + 4096u * i + (oW ^ (32u * (4u * dh + d))), pack4(qa[d][i], QSCALE));
    }
  }
};

template <int NQ>
DI void attention_role(const Params& p, Ctx& c, bf16x8 (&park)[16]) {
  LAS char* L = c.L;
  const u32 w = c.w, wave = c.wave; (void)wave;
  const int ln = lane_id();
  const u32 cl = ln & 15, g = ln >> 4, q4 = cl >> 2, p4 = ln & 3;
  const u32 pi_ = (cl & 3) | ((cl & 4) << 1) | ((cl & 8) >> 1);
  const u32 vk0 = 256u * g + 16u * (cl ^ g), vk1 = vk0 ^ 64u, vk2 = vk0 ^ 128u, vk3 = vk0 ^ 192u;
  const u32 kA = 256u * pi_ + 16u * (g ^ pi_);
  const u32 vA = 256u * (8u * (g & 1) + 4u * (g >> 1) + q4) + 16u * ((2u * (q4 + 4u * (g & 1))) | (p4 >> 1)) + 8u * (p4 & 1);
  const u32 rowq = L_QH + 256u * (32u * w + cl);
  const u32 qA = rowq + 16u * (g ^ cl);
  const u32 oW = 16u * ((g >> 1) ^ cl) + 8u * (g & 1);
  u64 d0 = 0;
  {
    const u32 b = c.b, j = c.j; (void)j; (void)b;
    const __amdgpu_buffer_rsrc_t srdW = mksrd(p.wq[c.br], DM * NH * DH * 2);
    const u32 vo_w = wq_pattern(c, ln);
    const u32 dh1 = DROT ^ 1u;
    Proj<NQ> pj;
    pj.init(c, ln);
    if (NQ == 1) d0 = ld_agent((const char*)p.ws + WS_DONE + 128u * member_bid(c, ln & 7));
    issue_x_rows(p, c, ln); issue_wq(c, srdW, vo_w, 0, 4, 8 * SLOT);
    barrier_vm<4>();
    if (NQ == 1) PIN(d0);
    pj.read_x(c, ln);
    barrier_vm<0>();
    issue_wq(c, srdW, vo_w, 0, 5, 3 * SLOT); issue_wq(c, srdW, vo_w, 0, 6, 4 * SLOT);
    pj.template piece<PFP>(c, 0, 8 * SLOT, dh1);
    barrier_vm<4>();
    issue_wq(c, srdW, vo_w, 0, 7, 2 * SLOT);
    pj.template piece<PFP>(c, 1, 3 * SLOT, dh1);
    issue_kv(c, c.srdK, 0, L_K, vk0, vk1, vk2, vk3);
    barrier_vm<8>();
    pj.template piece<PFP>(c, 2, 4 * SLOT, dh1);
    issue_kv(c, c.srdK, 1, L_K + SLOT, vk0, vk1, vk2, vk3);
    barrier_vm<8>();
    pj.template piece<PFP>(c, 3, 2 * SLOT, dh1);
    barrier_vm<4>();
  }
  issue_kv(c, c.srdK, 2, L_K + 2 * SLOT, vk0, vk1, vk2, vk3);
  bf16x8 kf[16];
  #pragma unroll
  for (int i = 0; i < 16; ++i) kf[i] = lds_b128(L, L_K + (kA ^ (64u * (i & 3))) + 4096u * (i >> 2));
  barrier_vm<4>();
  STAMP(1); STAMPS(0);
  bf16x8 qf[NQ][4];
  if (NQ == 1) {
    #pragma unroll
    for (int i = 0; i < 16; ++i) park[i] = bf16x8{};
  }
  bf16x8 ones = __builtin_bit_cast(bf16x8, u32x4{0x3F803F80u, 0x3F803F80u, 0x3F803F80u, 0x3F803F80u});
  PIN(ones);
  u32 s3 = 0;
  f32x4 O[8][NQ], negm[NQ], l[NQ];
  float m[NQ];
  #pragma unroll
  for (int i = 0; i < NQ; ++i) {
    m[i] = 0.f; negm[i] = f32x4{}; l[i] = f32x4{1.f, 1.f, 1.f, 1.f};
    #pragma unroll
    for (int d = 0; d < 8; ++d) O[d][i] = f32x4{};
  }
  TICKDECL
  auto step = [&](auto first_tag, const int rd, const int t) {
    constexpr bool FIRST = decltype(first_tag)::value;
    const u32 s3n = (s3 == 2) ? 0 : s3 + 1;
    const u32 vbase = L_V + s3 * SLOT, kbase = L_K + s3n * SLOT;
    const u32 sK = 8u * rd + t + 3;
    const bool kdma = FIRST || sK < 32;
    const u32 kdst = L_K + s3 * SLOT;
    TICK(0);
    float inv[NQ];
    if (FIRST) {
      #pragma unroll
      for (int i = 0; i < NQ; ++i)
        #pragma unroll
        for (int ks = 0; ks < 4; ++ks) qf[i][ks] = lds_b128(L, (qA ^ (64u * ks)) + 4096u * i);
      #pragma unroll
      for (int i = 0; i < NQ; ++i) inv[i] = __builtin_amdgcn_rcpf(l[i][0]);
      if (NQ == 1 && rd == 3) {
        if (ln < 8) { *lp<u64>(L_FLAG + 8u * ln) = d0; st_agent((char*)p.ws + WS_ACK + 128u * c.bid + 8u * ln, d0 ^ ACK_MIX); }
      }
    }
    if (KPOS == 0 && kdma) issue_kv(c, c.srdK, sK, kdst, vk0, vk1, vk2, vk3);
    if (WSPLIT && NQ == 1 && rd < 3) {
      const u32 s1 = 8u * rd + t + 1;
      const int l2 = lane_id();
      const u32 voe = 8192u * (l2 >> 3) + 16u * ((l2 & 7) ^ (2u * ((l2 >> 4) & 1)));
      const __amdgpu_buffer_rsrc_t srdW = mksrd(p.wq[c.br], DM * NH * DH * 2);
      issue_wq_quarter(c, srdW, voe, (1 + (s1 >> 3)) & 3, s1 & 7, L_W + ((s1 + 1) & 1) * SLOT, 0);
      issue_wq_quarter(c, srdW, voe, (1 + (s1 >> 3)) & 3, s1 & 7, L_W + ((s1 + 1) & 1) * SLOT, 1);
    }
    if (!FIRST && sK == 34) { const int l2 = lane_id(); issue_wo(p, c, l2, 0, 0); issue_wo(p, c, l2, 1, 1); }
    TICK(1);
    if (!FIRST && NQ == 1 && rd == 1 && t == 1) {
      #pragma unroll
      for (int i = 0; i < 16; ++i) park[i] = lds_b128(L, ((L_QH + 256u * cl + 16u * (g ^ cl)) ^ (64u * (i & 3))) + 4096u * ((i >> 2) == 3 ? 6 : (i >> 2)));
    }
    u32 va[8];
    va[0] = vbase + vA;
    #pragma unroll
    for (int d = 1; d < 8; ++d) va[d] = va[0] ^ (32u * d);
    auto vread = [&](const int d, const int u) { return lds_tr2(L, va[d] + 8192u * u, va[d] + 8192u * u + 4096u); };
    if (FIRST) { waitlgkm<0>(); SB(); }
    f32x4 S[4][NQ];
    bf16x8 vf[16];
    #pragma unroll
    for (int kt = 0; kt < 2; ++kt)
      #pragma unroll
      for (int ks = 0; ks < 4; ++ks) {
        #pragma unroll
        for (int i = 0; i < NQ; ++i) { if (ks == 0) MFMA_INIT(S[kt][i], kf[4 * kt + ks], qf[i][ks], negm[i]); else MFMA_ACC(S[kt][i], kf[4 * kt + ks], qf[i][ks]); }
        vf[4 * kt + ks] = vread(4 * kt + ks, 0);
        if (FIRST) {
          const int d = 4 * kt + ks;
          #pragma unroll
          for (int i = 0; i < NQ; ++i) lds_w64(L, rowq + 4096u * i + (oW ^ (32u * d)), pack4(O[d][i], inv[i]));
        }
        SB();
      }
    u32x4 pw[NQ][2];
    float pa[NQ];
    #pragma unroll
    for (int kt = 2; kt < 4; ++kt)
      #pragma unroll
      for (int ks = 0; ks < 4; ++ks) {
        const int n = 4 * (kt - 2) + ks;
        #pragma unroll
        for (int i = 0; i < NQ; ++i) { if (ks == 0) MFMA_INIT(S[kt][i], kf[4 * kt + ks], qf[i][ks], negm[i]); else MFMA_ACC(S[kt][i], kf[4 * kt + ks], qf[i][ks]); }
        vf[8 + n] = vread(n, 1);
        if (n >= 2 && n < 6) {
          const int k2 = (n - 2) >> 1, e = 2 * ((n - 2) & 1);
          #pragma unroll
          for (int i = 0; i < NQ; ++i) {
            const float x0 = S[k2][i][e], x1 = S[k2][i][e + 1];
            pa[i] = (n == 2) ? __builtin_elementwise_maximum(x0, x1) : vmax3(pa[i], x0, x1);
            u32 pk = cvtpk(EXP2(x0), EXP2(x1)); PIN(pk);
            pw[i][0][2 * k2 + (e >> 1)] = pk;
          }
        }
        SB();
      }
    asm volatile("" ::: "memory");
    NOP8();
    SB();
    TICK(2);
    #pragma unroll
    for (int i = 0; i < NQ; ++i) {
      const float pb = vmax3(S[2][i][0], S[2][i][1], S[2][i][2]);
      pa[i] = vmax3(pa[i], S[2][i][3], S[3][i][0]); pa[i] = vmax3(pa[i], S[3][i][1], S[3][i][2]);
      pa[i] = vmax3(pa[i], pb, S[3][i][3]);
    }
    bool move;
    if (FIRST) {
      move = false;
      #pragma unroll
      for (int i = 0; i < NQ; ++i) { pa[i] = xg_max(pa[i]); move |= !(__builtin_fabsf(pa[i]) <= THR); }
    } else move = (NQ == 2 ? __builtin_elementwise_maximum(pa[0], pa[NQ - 1]) : pa[0]) > THR;
    if (__builtin_expect(__any(move), 0)) {
      #pragma unroll
      for (int i = 0; i < NQ; ++i) {
        const float delta = FIRST ? pa[i] : fmaxf(xg_max(pa[i]), 0.f);
        m[i] += delta;
        negm[i] = f32x4{-m[i], -m[i], -m[i], -m[i]};
        #pragma unroll
        for (int kt = 0; kt < 4; ++kt) S[kt][i] -= delta;
        #pragma unroll
        for (int k2 = 0; k2 < 2; ++k2)
          #pragma unroll
          for (int e = 0; e < 4; e += 2) pw[i][0][2 * k2 + (e >> 1)] = cvtpk(EXP2(S[k2][i][e]), EXP2(S[k2][i][e + 1]));
        if (!FIRST) {
          const float alpha = EXP2(-delta);
          l[i] *= alpha;
          #pragma unroll
          for (int d = 0; d < 8; ++d) O[d][i] *= alpha;
        }
      }
      SB();
      NOP2();
    }
    if (KPOS == 1 && kdma) issue_kv(c, c.srdK, sK, kdst, vk0, vk1, vk2, vk3);
    waitlgkm<15>();
    SB();
    TICK(3);
    #pragma unroll
    for (int i = 0; i < NQ; ++i) { if (FIRST) MFMA_ZERO(l[i], ones, __builtin_bit_cast(bf16x8, pw[i][0])); else MFMA_ACC(l[i], ones, __builtin_bit_cast(bf16x8, pw[i][0])); }
    #pragma unroll
    for (int d = 0; d < 8; ++d) {
      #pragma unroll
      for (int i = 0; i < NQ; ++i) { if (FIRST) MFMA_ZERO(O[d][i], vf[d], __builtin_bit_cast(bf16x8, pw[i][0])); else MFMA_ACC(O[d][i], vf[d], __builtin_bit_cast(bf16x8, pw[i][0])); }
      if (d < 4) {
        const int k2 = 2 + (d >> 1), e = 2 * (d & 1);
        #pragma unroll
        for (int i = 0; i < NQ; ++i) { u32 pk = cvtpk(EXP2(S[k2][i][e]), EXP2(S[k2][i][e + 1])); PIN(pk); pw[i][1][2 * (k2 - 2) + (e >> 1)] = pk; }
      }
      SB();
    }
    u32 ka[4];
    ka[0] = kbase + kA;
    #pragma unroll
    for (int ks = 1; ks < 4; ++ks) ka[ks] = ka[0] ^ (64u * ks);
    waitlgkm<0>();
    SB();
    #pragma unroll
    for (int i = 0; i < NQ; ++i) MFMA_ACC(l[i], ones, __builtin_bit_cast(bf16x8, pw[i][1]));
    #pragma unroll
    for (int d = 0; d < 8; ++d) {
      #pragma unroll
      for (int i = 0; i < NQ; ++i) MFMA_ACC(O[d][i], vf[8 + d], __builtin_bit_cast(bf16x8, pw[i][1]));
      SB();
      kf[2 * d] = lds_b128(L, ka[(2 * d) & 3] + 4096u * ((2 * d) >> 2));
      kf[2 * d + 1] = lds_b128(L, ka[(2 * d + 1) & 3] + 4096u * ((2 * d + 1) >> 2));
      SB();
    }
    s3 = s3n;
    TICK(4);
    if (FIRST && NQ == 1 && rd == 3) barrier_vm<5>();
    else if (kdma) barrier_vm<4>(); else barrier_vm<0>();
    TICK(5);
    STAMPS(8 * rd + t + 1);
  };
  #pragma unroll 1
  for (int rd = 0; rd < 4; ++rd) {
    step(std::true_type{}, rd, 0);
    #pragma unroll 1
    for (int t = 1; t < 8; ++t) step(std::false_type{}, rd, t);
    STAMP(2 + rd);
  }
  NOP8();
  SB();
  #pragma unroll
  for (int i = 0; i < NQ; ++i) {
    const float inv = __builtin_amdgcn_rcpf(l[i][0]);
    #pragma unroll
    for (int d = 0; d < 8; ++d) lds_w64(L, rowq - L_QH + 4096u * i + (oW ^ (32u * d)), pack4(O[d][i], inv));
  }
  TICKOUT
}

template <int NQ>
DI void projection_role(const Params& p, Ctx& c, bf16x8 (&park)[16]) {
  LAS char* L = c.L;
  const u32 w = c.w, wave = c.wave; (void)wave;
  const u32 b = c.b, j = c.j; (void)j;
  const int ln = lane_id();
  const u32 cl = ln & 15, g = ln >> 4, q4 = cl >> 2, p4 = ln & 3;
  const u32 vv0 = 256u * g + 16u * (cl ^ (2u * g)), vv2 = vv0 ^ 128u;
  const __amdgpu_buffer_rsrc_t srdW = mksrd(p.wq[c.br], DM * NH * DH * 2);
  const u32 rot = KROT, dh0 = DROT;
  const u32 vo_w = wq_pattern(c, ln);
  auto issue_w = [&](u32 rdn, u32 pi, u32 la) { issue_wq(c, srdW, vo_w, rdn, pi, la); };
  Proj<NQ> pj;
  pj.init(c, ln);
  const u32 rowq = pj.rowq;
  const u32 qA = rowq + 16u * (g ^ cl);
  STAMP(13);
  issue_w(0, 0, 7 * SLOT); issue_x_rows(p, c, ln); issue_w(0, 1, 9 * SLOT);
  STAMP(14);
  barrier_vm<4>();
  STAMP(1);
  pj.read_x(c, ln);
  barrier_lgkm();
  issue_w(0, 2, 5 * SLOT);
  pj.template piece<PFP>(c, 0, 7 * SLOT, dh0);
  barrier_vm<4>();
  STAMP(2);
  issue_w(0, 3, 6 * SLOT);
  pj.template piece<PFP>(c, 1, 9 * SLOT, dh0);
  barrier_vm<4>();
  issue_w(1, 0, 7 * SLOT);
  pj.template piece<PFP>(c, 2, 5 * SLOT, dh0);
  issue_kv(c, c.srdV, 0, L_V, vv0, vv0, vv2, vv2);
  barrier_vm<8>();
  STAMP(3);
  issue_kv(c, c.srdV, 1, L_V + SLOT, vv0, vv0, vv2, vv2);
  pj.template piece<PFP>(c, 3, 6 * SLOT, dh0);
  barrier_lgkm();
  STAMP(4);
  barrier_vm<4>();
  STAMP(5);
  bf16x8 og1[NQ][4];
  #pragma unroll
  for (int i = 0; i < NQ; ++i)
    #pragma unroll
    for (int ks = 0; ks < 4; ++ks) og1[i][ks] = bf16x8{};
  if (NQ == 1) {
    #pragma unroll
    for (int i = 4; i < 16; ++i) park[i] = bf16x8{};
  }
  u32 s3 = 0;
  TICKDECL
  #pragma unroll 1
  for (int hh = 0; hh < 6; ++hh) {
    #pragma unroll
    for (int kq = 0; kq < 4; ++kq) {
      const u32 s = 4u * hh + kq, s1 = s + 1;
      const u32 vdst = L_V + ((s3 == 0) ? 2 : s3 - 1) * SLOT;
      TICK(0);
      if (!WSPLIT) issue_w((1 + (s1 >> 3)) & 3, s1 & 7, L_W + (kq & 1) * SLOT);
      else if (NQ == 1) {
        issue_wq_quarter(c, srdW, vo_w ^ 64u, (1 + (s1 >> 3)) & 3, s1 & 7, L_W + (kq & 1) * SLOT, 2);
        issue_wq_quarter(c, srdW, vo_w ^ 64u, (1 + (s1 >> 3)) & 3, s1 & 7, L_W + (kq & 1) * SLOT, 3);
      }
      if (VEARLY2 && NQ == 2) issue_kv(c, c.srdV, s + 2, vdst, vv0, vv0, vv2, vv2);
      TICK(1);
      if (NQ == 1 && kq == 1 && hh == 2) {
        #pragma unroll
        for (int i = 4; i < 16; ++i) park[i] = lds_b128(L, ((L_QH + 256u * cl + 16u * (g ^ cl)) ^ (64u * (i & 3))) + 4096u * (2 + (i >> 2)));
      }
      if (kq == 1 && hh == 4) {
        #pragma unroll
        for (int i = 0; i < NQ; ++i)
          #pragma unroll
          for (int ks = 0; ks < 4; ++ks) og1[i][ks] = lds_b128(L, (qA ^ (64u * ks)) + 4096u * i);
      }
      pj.piece(c, kq, L_W + ((kq + 1) & 1) * SLOT, (u32)(hh & 1) ^ dh0);
      if (!(VEARLY2 && NQ == 2)) issue_kv(c, c.srdV, s + 2, vdst, vv0, vv0, vv2, vv2);
      s3 = (s3 == 2) ? 0 : s3 + 1;
      TICK(2);
      barrier_vm<4>();
      TICK(4);
    }
  }
  #pragma unroll
  for (int i = 0; i < 4 * NQ; ++i) park[i] = bf16x8{};
  const __amdgpu_buffer_rsrc_t srdO = mksrd(p.wo[c.br], NH * DH * DM * 2);
  #pragma unroll 1
  for (u32 s = 24; s < 32; ++s) {
    const u32 vdst = L_V + ((s3 == 0) ? 2 : s3 - 1) * SLOT;
    if (s + 2 < 32) issue_kv(c, c.srdV, s + 2, vdst, vv0, vv0, vv2, vv2);
    if (s == WARMS) {
      #pragma unroll
      for (int i = 0; i < 8; ++i) dma_w(rfl(c.ldsb + L_W + 1024u * w + 4096u * i), 16u * ln, srdO, rfl((c.hg * 512u + 32u * b + 4u * i + w) * 1024u));
    }
    if (s == 31) { issue_wo(p, c, ln, 0, 0); issue_wo(p, c, ln, 1, 1); }
    if (s == 25) {
      #pragma unroll
      for (int i = 0; i < NQ; ++i)
        #pragma unroll
        for (int ks = 0; ks < 4; ++ks) park[4 * i + ks] = lds_b128(L, (qA ^ (64u * ks)) + 4096u * i);
    }
    s3 = (s3 == 2) ? 0 : s3 + 1;
    if (s >= 30) barrier_vm<0>(); else if (s == WARMS || s == WARMS + 1) barrier_vm<12>(); else barrier_vm<4>();
  }
  STAMP(6);
  TICKOUT
  #pragma unroll
  for (int i = 0; i < NQ; ++i)
    #pragma unroll
    for (int ks = 0; ks < 4; ++ks) lds_w128(L, OREG + ((qA - L_QH) ^ (64u * ks)) + 4096u * i, og1[i][ks]);
}

__global__ __launch_bounds__(512, 2) void fused_attn(Params p) {
  extern __shared__ __attribute__((aligned(16))) char smem[];
  Ctx c;
  c.L = (LAS char*)smem;
  c.ldsb = (u32)(size_t)smem;
  c.wave = rfl(threadIdx.x >> 6);
  c.w = c.wave & 3;
  const u32 wave = c.wave, w = c.w;
  const u32 bid = blockIdx.x, xcd = bid & 7;
  c.j = bid >> 3; c.bid = bid;
  c.br = xcd & 1; c.hg = 2 * (xcd >> 1) + (c.j & 1); c.b = c.j >> 1;
  const size_t kvoff = (size_t)((c.b * NH + c.hg * 4) * SK) * DH * 2;
  c.srdK = mksrd((const char*)p.k[c.br] + kvoff, 4 * SK * DH * 2);
  c.srdV = mksrd((const char*)p.v[c.br] + kvoff, 4 * SK * DH * 2);
  STAMP(0); STAMPC(8);
  bf16x8 park[16];
  if (wave < 3) attention_role<2>(p, c, park);
  else if (wave == 3) attention_role<1>(p, c, park);
  else if (wave < 7) projection_role<2>(p, c, park);
  else projection_role<1>(p, c, park);

  {
    LAS char* L = c.L;
    const int ln = lane_id();
    const u32 cl = ln & 15, g = ln >> 4, q4 = cl >> 2, p4 = ln & 3;
    const char* ackp = (const char*)p.ws + WS_ACK + 128u * member_bid(c, ln & 7) + 8u * c.hg;
    u64 ack;
    asm volatile("global_load_dwordx2 %0, %1, off sc1" : "=&v"(ack) : "v"(ackp) : "memory");
    issue_wo(p, c, ln, 2, 2);
    barrier_lgkm();
    STAMP(7);
    const u32 oA = 256u * cl + 16u * (g ^ cl);
    const u32 wA = 128u * (8u * g + q4) + 16u * ((2u * ((q4 >> 1) + 2u * (g & 1))) | (p4 >> 1)) + 8u * (p4 & 1);
    auto aread = [&](bf16x8 (&af)[4], u32 slot3) {
      u32 rb = rfl(ring_slot(wave, slot3));
      OPAQUE_S(rb);
      const u32 a0 = rb + wA;
      #pragma unroll
      for (int nt = 0; nt < 4; ++nt) { const u32 a = a0 ^ (32u * nt); af[nt] = lds_tr2(L, a, a + 512u); }
    };
    f32x4 acc[4][7];
    bf16x8 afA[4], afB[4], bq[7];
    auto oaddr = [&](u32 u) { return OREG * ((u >> 2) & 1) + (oA ^ (64u * (u & 3))); };
    SB();
    aread(afA, 0);
    {
      const u32 ob = oaddr(0);
      #pragma unroll
      for (int qt = 0; qt < 4; ++qt) bq[qt] = lds_b128(L, ob + 4096u * qt);
    }
    u32 sl = 0;
    auto kstep = [&](const u32 u, bf16x8 (&af)[4], bf16x8 (&afn)[4]) {
      const u32 sl1 = (sl == 2) ? 0 : sl + 1;
      const u32 ob = oaddr(u), obn = oaddr(u + 1);
      const bool pre = (u & 7) != 7;
      #pragma unroll
      for (int qt = 0; qt < 7; ++qt) {
        #pragma unroll
        for (int nt = 0; nt < 4; ++nt) { if (u == 0) MFMA_ZERO(acc[nt][qt], af[nt], bq[qt]); else MFMA_ACC(acc[nt][qt], af[nt], bq[qt]); }
        SB();
        if (qt < 3) bq[qt + 4] = lds_b128(L, ob + 4096u * (qt + 4));
        else if (pre) bq[qt - 3] = lds_b128(L, obn + 4096u * (qt - 3));
        if (qt == 0) issue_wo(p, c, ln, u + 3, sl);
        if (qt == 2) { waitvm<8>(); if (u == 0) asm volatile("" : "+v"(ack)); aread(afn, sl1); }
        SB();
      }
      sl = sl1;
    };
    #pragma unroll
    for (u32 u = 0; u < 16; u += 2) {
      if (u == 8) {
        barrier_lgkm();
        if (wave == 3 || wave == 7) {
          #pragma unroll
          for (int i = 0; i < 16; ++i) {
            if (wave == 3) lds_w128(L, (oA ^ (64u * (i & 3))) + 4096u * ((i >> 2) == 3 ? 6 : (i >> 2)), park[i]);
            else if (i >= 4) lds_w128(L, (oA ^ (64u * (i & 3))) + 4096u * (2 + (i >> 2)), park[i]);
          }
        }
        if (wave >= 4) {
          #pragma unroll
          for (int ks = 0; ks < 4; ++ks) {
            lds_w128(L, OREG + (oA ^ (64u * ks)) + 8192u * w, park[ks]);
            if (w < 3) lds_w128(L, OREG + (oA ^ (64u * ks)) + 8192u * w + 4096u, park[4 + ks]);
          }
        }
        barrier_lgkm();
        const u32 ob = oaddr(8);
        #pragma unroll
        for (int qt = 0; qt < 4; ++qt) bq[qt] = lds_b128(L, ob + 4096u * qt);
      }
      kstep(u, afA, afB);
      kstep(u + 1, afB, afA);
    }
    waitvm<0>();
    STAMP(11);
    NOP8();
    SB();
    const __amdgpu_buffer_rsrc_t srdP = mksrd((const char*)p.ws + WS_P + (size_t)(c.br * NB + c.b) * 8 * PART_WG, 8 * PART_WG);
    #pragma unroll
    for (int qt = 0; qt < 7; ++qt)
      #pragma unroll
      for (int np = 0; np < 2; ++np) {
        const u32x2 a = pack4n(acc[2 * np][qt]), bb = pack4n(acc[2 * np + 1][qt]);
        __builtin_amdgcn_raw_buffer_store_b128(u32x4{a[0], a[1], bb[0], bb[1]}, srdP, c.hg * PART_WG + wave * 14336u + (qt * 2 + np) * 1024u + ln * 16u, 0, 16);
      }
    waitvm<0>();
    barrier_lgkm();
    STAMP(11);
    if (wave == 0) {
      const u64 e0 = *lp<u64>(L_FLAG + 8u * (ln & 7));
      const u64 want = *lp<u64>(L_FLAG + 8u * c.hg) ^ ACK_MIX;
      for (u32 spin = 0; !__all(ack == want) && spin < SPIN_CAP; ++spin) { __builtin_amdgcn_s_sleep(8); ack = ld_agent(ackp); }
      if (ln == (int)c.hg) st_agent((char*)p.ws + WS_DONE + 128u * c.bid, e0 + 1);
      const char* dp = (const char*)p.ws + WS_DONE + 128u * member_bid(c, ln & 7);
      for (u32 spin = 0; spin < SPIN_CAP; ++spin) { if (__all(ld_agent(dp) == e0 + 1)) break; __builtin_amdgcn_s_sleep(2); }
    }
    barrier_lgkm();
    STAMP(12);
    if (wave < 7) {
      u32x4 x[2][8];
      #pragma unroll
      for (int np = 0; np < 2; ++np)
        #pragma unroll
        for (int m = 0; m < 8; ++m) x[np][m] = __builtin_amdgcn_raw_buffer_load_b128(srdP, m * PART_WG + c.hg * 14336u + (wave * 2 + np) * 1024u + ln * 16u, 0, 16);
      const u32 tile = ring_slot(wave, 0);
      #pragma unroll
      for (int np = 0; np < 2; ++np) {
        float s8[8] = {};
        #pragma unroll
        for (int m = 0; m < 8; ++m)
          #pragma unroll
          for (int i = 0; i < 4; ++i) { s8[2 * i] += __uint_as_float(x[np][m][i] << 16); s8[2 * i + 1] += __uint_as_float(x[np][m][i] & 0xffff0000u); }
        lds_w64(L, tile + cl * 128u + 64u * np + 8u * g, u32x2{cvtpk(s8[0], s8[1]), cvtpk(s8[2], s8[3])});
        lds_w64(L, tile + cl * 128u + 64u * np + 32u + 8u * g, u32x2{cvtpk(s8[4], s8[5]), cvtpk(s8[6], s8[7])});
      }
      #pragma unroll
      for (int i = 0; i < 2; ++i) {
        const u32 row = 8u * i + (ln >> 3), ch = ln & 7;
        char* o = (char*)p.out + (((size_t)c.b * SQ + 16u * wave + row) * (2 * DM) + c.br * DM + 64u * c.hg) * 2 + 16u * ch;
        *(bf16x8*)o = lds_b128(L, tile + row * 128u + 16u * ch);
      }
    }
  }
  STAMP(10); STAMPC(9);
  STAMPOUT();
}

extern "C" void kernel_launch(void* const* d_in, const int* in_sizes, int n_in, void* d_out, int out_size, void* d_ws, size_t ws_size,
                              hipStream_t stream) {
  Params p;
  for (int i = 0; i < 2; ++i) { p.x[i] = d_in[5 * i]; p.k[i] = d_in[5 * i + 1]; p.v[i] = d_in[5 * i + 2]; p.wq[i] = d_in[5 * i + 3]; p.wo[i] = d_in[5 * i + 4]; }
  p.out = d_out; p.ws = d_ws;
  (void)hipFuncSetAttribute((const void*)fused_attn, hipFuncAttributeMaxDynamicSharedMemorySize, LDS_BYTES);
  fused_attn<<<256, 512, LDS_BYTES, stream>>>(p);
}
```

```cpp
#include <hip/hip_runtime.h>
#include <hip/hip_bf16.h>
#include <type_traits>

typedef unsigned u32;
typedef unsigned long long u64;
typedef __bf16 bf16x8 __attribute__((ext_vector_type(8)));
typedef __bf16 bf16x4 __attribute__((ext_vector_type(4)));
typedef __bf16 bf16x2 __attribute__((ext_vector_type(2)));
typedef float f32x4 __attribute__((ext_vector_type(4)));
typedef float f32x2 __attribute__((ext_vector_type(2)));
typedef u32 u32x4 __attribute__((ext_vector_type(4)));
typedef u32 u32x2 __attribute__((ext_vector_type(2)));
#define LAS __attribute__((address_space(3)))
#define DI __device__ __forceinline__

constexpr int NB = 16, SQ = 112, DM = 512, NH = 32, SK = 512, DH = 128;
constexpr float QSCALE = 0.12751743082f;
constexpr float THR = 8.0f;

constexpr u32 SLOT = 16384;
constexpr u32 L_K = 0;
constexpr u32 L_V = 3 * SLOT;
constexpr u32 L_W = 6 * SLOT;
constexpr u32 L_QH = 8 * SLOT;
constexpr u32 LDS_BYTES = 10 * SLOT;
constexpr u32 OREG = 28672;
constexpr u32 PIECE = 4096;
constexpr u32 L_RS0 = 90112, L_RS1 = 122880, L_RS2 = 2 * OREG;

constexpr size_t WS_P = 0;
constexpr u32 PART_WG = 8 * 7 * 2 * 1024;
constexpr size_t WS_DONE = 32u << 20, WS_ACK = WS_DONE + 65536;
constexpr u32 L_FLAG = 159744;
constexpr unsigned long long ACK_MIX = 0x9E3779B97F4A7C15ull;
constexpr u32 SPIN_CAP = 1u << 18;

struct Params {
  const void* x[2]; const void* k[2]; const void* v[2]; const void* wq[2]; const void* wo[2];
  void* out; void* ws;
};

DI u32 rfl(u32 x) { return __builtin_amdgcn_readfirstlane(x); }
DI __amdgpu_buffer_rsrc_t mksrd(const void* p, u32 bytes) { return __builtin_amdgcn_make_buffer_rsrc((void*)p, (short)0, (int)bytes, 0x00020000); }
DI void dma_nt(u32 m0v, u32 voff, __amdgpu_buffer_rsrc_t srd, u32 soff) {
  asm volatile("s_mov_b32 m0, %0\n\ts_nop 0\n\tbuffer_load_dwordx4 %1, %2, %3 offen nt lds" :: "s"(m0v), "v"(voff), "s"(srd), "s"(soff) : "memory");
}
DI void dma(u32 m0v, u32 voff, __amdgpu_buffer_rsrc_t srd, u32 soff) {
  asm volatile("s_mov_b32 m0, %0\n\ts_nop 0\n\tbuffer_load_dwordx4 %1, %2, %3 offen lds" :: "s"(m0v), "v"(voff), "s"(srd), "s"(soff) : "memory");
}
constexpr int PF = 3;
constexpr int PFP = 6;
constexpr u32 WARMS = 29;
#define KROT (((b >> 2) + 2 * (j & 1)) & 3)
#define DROT ((b >> 1) & 1)

template <int N> DI void waitvm() { asm volatile("s_waitcnt vmcnt(%0)" :: "i"(N) : "memory"); }
template <int N> DI void waitlgkm() { __builtin_amdgcn_s_waitcnt(0xC07F | (N << 8)); }
DI void barrier_lgkm() { asm volatile("" ::: "memory"); waitlgkm<0>(); __builtin_amdgcn_s_barrier(); asm volatile("" ::: "memory"); }
template <int N> DI void barrier_vm() { asm volatile("s_waitcnt vmcnt(%0)" :: "i"(N) : "memory"); waitlgkm<0>(); __builtin_amdgcn_s_barrier(); asm volatile("" ::: "memory"); }
DI u32 cvtpk(float lo, float hi) { f32x2 v = {lo, hi}; bf16x2 b = __builtin_convertvector(v, bf16x2); return __builtin_bit_cast(u32, b); }
DI u32x2 pack4(const f32x4& a, float s) { return u32x2{cvtpk(a[0] * s, a[1] * s), cvtpk(a[2] * s, a[3] * s)}; }
DI u32x2 pack4n(const f32x4& a) { return u32x2{cvtpk(a[0], a[1]), cvtpk(a[2], a[3])}; }
template <class T> DI LAS T* lp(u32 off) { return (LAS T*)off; }
DI bf16x8 lds_b128(LAS char*, u32 off) { return *lp<bf16x8>(off); }
DI void lds_w128(LAS char*, u32 off, bf16x8 v) { *lp<bf16x8>(off) = v; }
DI void lds_w64(LAS char*, u32 off, u32x2 v) { *lp<u32x2>(off) = v; }
DI bf16x8 lds_tr2(LAS char*, u32 off0, u32 off1) {
  bf16x4 a = __builtin_amdgcn_ds_read_tr16_b64_v4bf16(lp<bf16x4>(off0));
  bf16x4 b = __builtin_amdgcn_ds_read_tr16_b64_v4bf16(lp<bf16x4>(off1));
  return __builtin_shufflevector(a, b, 0, 1, 2, 3, 4, 5, 6, 7);
}
#define SB() __builtin_amdgcn_sched_barrier(0)
#define PIN(x) asm volatile("" : "+v"(x))
#define OPAQUE_S(x) asm volatile("" : "+s"(x))
DI float vmax3(float a, float b, float c) { return __builtin_elementwise_maximum(__builtin_elementwise_maximum(a, b), c); }
#define MFMA_ACC(acc, a, b) asm volatile("v_mfma_f32_16x16x32_bf16 %0, %1, %2, %0" : "+v"(acc) : "v"(a), "v"(b))
#define MFMA_INIT(acc, a, b, c) asm volatile("v_mfma_f32_16x16x32_bf16 %0, %1, %2, %3" : "=&v"(acc) : "v"(a), "v"(b), "v"(c))
#define MFMA_ZERO(acc, a, b) asm volatile("v_mfma_f32_16x16x32_bf16 %0, %1, %2, 0" : "=&v"(acc) : "v"(a), "v"(b))
#define NOP8() asm volatile("s_nop 7")
#define NOP2() asm volatile("s_nop 1")
#define EXP2(x) __builtin_amdgcn_exp2f(x)
#define GAS __attribute__((address_space(1)))
DI u64 ld_agent(const char* p) { return __hip_atomic_load((const GAS u64*)p, __ATOMIC_RELAXED, __HIP_MEMORY_SCOPE_AGENT); }
DI void st_agent(char* p, u64 v) { __hip_atomic_store((GAS u64*)p, v, __ATOMIC_RELAXED, __HIP_MEMORY_SCOPE_AGENT); }
DI int lane_id() { int ln; asm volatile("v_mbcnt_lo_u32_b32 %0, -1, 0\n\tv_mbcnt_hi_u32_b32 %0, -1, %0" : "=v"(ln)); return ln; }
DI float xg_max(float x) {
  auto a = __builtin_amdgcn_permlane16_swap(__float_as_uint(x), __float_as_uint(x), false, false);
  x = fmaxf(__uint_as_float(a[0]), __uint_as_float(a[1]));
  auto b = __builtin_amdgcn_permlane32_swap(__float_as_uint(x), __float_as_uint(x), false, false);
  return fmaxf(__uint_as_float(b[0]), __uint_as_float(b[1]));
}

struct Ctx {
  LAS char* L; u32 ldsb, wave, w, br, b, hg, j, bid;
  __amdgpu_buffer_rsrc_t srdK, srdV;
};

DI u32 member_bid(const Ctx& c, u32 m) { return 8u * (2u * c.b + (m & 1u)) + c.br + 2u * (m >> 1); }

DI u32 ring_slot(u32 wave, u32 k) { return (k == 0 ? L_RS0 : k == 1 ? L_RS1 : L_RS2) + wave * PIECE; }
DI void issue_wo(const Params& p, const Ctx& c, int ln, u32 u, u32 k) {
  const __amdgpu_buffer_rsrc_t srdO = mksrd(p.wo[c.br], NH * DH * DM * 2);
  const u32 vo0 = 1024u * (ln >> 3) + 16u * ((ln & 7) ^ (2u * ((ln >> 4) & 1))), vo1 = vo0 ^ 64u;
  const u32 hs = (0x2013u >> (4u * ((u >> 2) & 3))) & 3u;
  const u32 so = (u < 16) ? ((c.hg * 4 + hs) * 128u + 32u * (u & 3)) * 1024u + 128u * c.wave : 0x01000000u;
  const u32 lb = c.ldsb + ring_slot(c.wave, k);
  dma(rfl(lb), vo0, srdO, rfl(so));                     dma(rfl(lb + 1024u), vo1, srdO, rfl(so + 8192u));
  dma(rfl(lb + 2048u), vo0, srdO, rfl(so + 16384u));     dma(rfl(lb + 3072u), vo1, srdO, rfl(so + 24576u));
}

DI void issue_kv(Ctx& c, const __amdgpu_buffer_rsrc_t srd, u32 st, u32 la, u32 vo0, u32 vo1, u32 vo2, u32 vo3) {
  asm volatile("s_mov_b32 m0, %0\n\ts_nop 0\n\t"
               "buffer_load_dwordx4 %1, %5, %6 offen nt lds\n\t"
               "buffer_load_dwordx4 %2, %5, %6 offen offset:1024 nt lds\n\t"
               "buffer_load_dwordx4 %3, %5, %6 offen offset:2048 nt lds\n\t"
               "buffer_load_dwordx4 %4, %5, %6 offen offset:3072 nt lds"
               :: "s"(rfl(c.ldsb + la + 4096u * c.w)), "v"(vo0), "v"(vo1), "v"(vo2), "v"(vo3), "s"(srd), "s"(rfl(st * SLOT + 4096u * c.w)) : "memory");
}

DI u32 wq_pattern(const Ctx& c, int ln) { return 8192u * (ln >> 3) + 16u * ((ln & 7) ^ (2u * (((ln >> 4) & 1) + 2u * (c.w & 1)))); }
DI void issue_wq_quarter(const Ctx& c, const __amdgpu_buffer_rsrc_t srdW, u32 vo_even, u32 rdn, u32 pi, u32 la, const u32 q) {
  const u32 b = c.b, j = c.j; (void)j; (void)b;
  const u32 hd = c.hg * 4 + rdn;
  const u32 kq = ((pi & 3) + KROT) & 3, dh = (pi >> 2) ^ DROT;
  const u32 so = (128u * kq + 8u * q) * 8192u + (128u * hd + 64u * dh) * 2u;
  const u32 vo = vo_even ^ (64u * (q & 1));
  #pragma unroll
  for (int i = 0; i < 4; ++i) dma(rfl(c.ldsb + la + 1024u * q + 4096u * i), vo, srdW, rfl(so + 32u * 8192u * i));
}
DI void issue_wq(const Ctx& c, const __amdgpu_buffer_rsrc_t srdW, u32 vo_w, u32 rdn, u32 pi, u32 la) {
  const u32 b = c.b, j = c.j; (void)j; (void)b;
  const u32 hd = c.hg * 4 + rdn;
  const u32 kq = ((pi & 3) + KROT) & 3, dh = (pi >> 2) ^ DROT;
  const u32 so = (128u * kq + 8u * c.w) * 8192u + (128u * hd + 64u * dh) * 2u;
  #pragma unroll
  for (int i = 0; i < 4; ++i) dma(rfl(c.ldsb + la + 1024u * c.w + 4096u * i), vo_w, srdW, rfl(so + 32u * 8192u * i));
}
DI void issue_x_rows(const Params& p, Ctx& c, int ln) {
  const __amdgpu_buffer_rsrc_t srdX = mksrd((const char*)p.x[c.br] + (size_t)c.b * SQ * DM * 2, SQ * DM * 2);
  const u32 start = 56u * (c.j & 1);
  auto one = [&](u32 row) {
    row += start; row = row >= 112u ? row - 112u : row;
    dma(rfl(c.ldsb + 1024u * row), (16u * ln) ^ rfl(16u * (row & 15u)), srdX, rfl(1024u * row));
  };
  if (c.wave < 4) {
    #pragma unroll
    for (int i = 0; i < 16; ++i) one(c.w + 4u * i);
  } else {
    #pragma unroll
    for (int i = 0; i < 12; ++i) one(64u + c.w + 4u * i);
  }
}

template <int NQ> struct Proj {
  bf16x8 xf[NQ][16];
  f32x4 qa[4][NQ];
  u32 wA, rowq, oW;
  DI void init(const Ctx& c, int ln) {
    const u32 cl = ln & 15, g = ln >> 4, q4 = cl >> 2, p4 = ln & 3;
    wA = 128u * (8u * g + q4) + 16u * ((2u * ((q4 >> 1) + 2u * (g & 1))) | (p4 >> 1)) + 8u * (p4 & 1);
    rowq = L_QH + 256u * (32u * c.w + cl);
    oW = 16u * ((g >> 1) ^ cl) + 8u * (g & 1);
  }
  DI void read_x(const Ctx& c, int ln) {
    const u32 b = c.b, j = c.j; (void)j; (void)b;
    const u32 cl = ln & 15, g = ln >> 4;
    const u32 xA = 1024u * (32u * c.w + cl) + 16u * (g ^ cl);
    #pragma unroll
    for (int jq = 0; jq < 4; ++jq) {
      const u32 a = xA + 256u * (((u32)jq + KROT) & 3u);
      #pragma unroll
      for (int i = 0; i < NQ; ++i)
        #pragma unroll
        for (int ks = 0; ks < 4; ++ks) xf[i][4 * jq + ks] = lds_b128(c.L, (a ^ (64u * ks)) + 16384u * i);
    }
  }
  template <int PFV = PF>
  DI void piece(const Ctx& c, const int kq, u32 la, u32 dh) {
    LAS char* L = c.L;
    OPAQUE_S(la);
    u32 wa[4];
    #pragma unroll
    for (int d = 0; d < 4; ++d) wa[d] = la + (wA ^ (32u * d));
    auto wread = [&](const int n) { return lds_tr2(L, wa[n & 3] + 4096u * (n >> 2), wa[n & 3] + 4096u * (n >> 2) + 512u); };
    bf16x8 fr[16];
    #pragma unroll
    for (int n = 0; n < 2 * PFV; ++n) fr[n] = wread(n);
    SB();
    #pragma unroll
    for (int hp = 0; hp < 8; ++hp) {
      const int young = 4 * ((7 - hp) < (PFV - 1) ? (7 - hp) : (PFV - 1));
      if (young >= 12) waitlgkm<12>(); else if (young == 8) waitlgkm<8>(); else if (young == 4) waitlgkm<4>(); else waitlgkm<0>();
      SB();
      #pragma unroll
      for (int e = 0; e < 2; ++e) {
        const int n = 2 * hp + e, ks = n >> 2, d = n & 3;
        #pragma unroll
        for (int i = 0; i < NQ; ++i) { if (kq == 0 && ks == 0) MFMA_ZERO(qa[d][i], fr[n], xf[i][4 * kq + ks]); else MFMA_ACC(qa[d][i], fr[n], xf[i][4 * kq + ks]); }
      }
      SB();
      if (hp + PFV < 8) { fr[2 * (hp + PFV)] = wread(2 * (hp + PFV)); fr[2 * (hp + PFV) + 1] = wread(2 * (hp + PFV) + 1); }
      SB();
    }
    if (kq == 3) {
      NOP8();
      SB();
      #pragma unroll
      for (int i = 0; i < NQ; ++i)
        #pragma unroll
        for (int d = 0; d < 4; ++d) lds_w64(L, rowq + 4096u * i + (oW ^ (32u * (4u * dh + d))), pack4(qa[d][i], QSCALE));
    }
  }
};

template <int NQ>
DI void attention_role(const Params& p, Ctx& c, bf16x8 (&park)[16]) {
  LAS char* L = c.L;
  const u32 w = c.w, wave = c.wave; (void)wave;
  const int ln = lane_id();
  const u32 cl = ln & 15, g = ln >> 4, q4 = cl >> 2, p4 = ln & 3;
  const u32 pi_ = (cl & 3) | ((cl & 4) << 1) | ((cl & 8) >> 1);
  const u32 vk0 = 256u * g + 16u * (cl ^ g), vk1 = vk0 ^ 64u, vk2 = vk0 ^ 128u, vk3 = vk0 ^ 192u;
  const u32 kA = 256u * pi_ + 16u * (g ^ pi_);
  const u32 vA = 256u * (8u * (g & 1) + 4u * (g >> 1) + q4) + 16u * ((2u * (q4 + 4u * (g & 1))) | (p4 >> 1)) + 8u * (p4 & 1);
  const u32 rowq = L_QH + 256u * (32u * w + cl);
  const u32 qA = rowq + 16u * (g ^ cl);
  const u32 oW = 16u * ((g >> 1) ^ cl) + 8u * (g & 1);
  u64 d0 = 0;
  {
    const u32 b = c.b, j = c.j; (void)j; (void)b;
    const __amdgpu_buffer_rsrc_t srdW = mksrd(p.wq[c.br], DM * NH * DH * 2);
    const u32 vo_w = wq_pattern(c, ln);
    const u32 dh1 = DROT ^ 1u;
    Proj<NQ> pj;
    pj.init(c, ln);
    if (NQ == 1) d0 = ld_agent((const char*)p.ws + WS_DONE + 128u * member_bid(c, ln & 7));
    issue_x_rows(p, c, ln); issue_wq(c, srdW, vo_w, 0, 4, 8 * SLOT);
    barrier_vm<4>();
    if (NQ == 1) PIN(d0);
    pj.read_x(c, ln);
    barrier_vm<0>();
    issue_wq(c, srdW, vo_w, 0, 5, 3 * SLOT); issue_wq(c, srdW, vo_w, 0, 6, 4 * SLOT);
    pj.template piece<PFP>(c, 0, 8 * SLOT, dh1);
    barrier_vm<4>();
    issue_wq(c, srdW, vo_w, 0, 7, 2 * SLOT);
    pj.template piece<PFP>(c, 1, 3 * SLOT, dh1);
    issue_kv(c, c.srdK, 0, L_K, vk0, vk1, vk2, vk3);
    barrier_vm<8>();
    pj.template piece<PFP>(c, 2, 4 * SLOT, dh1);
    issue_kv(c, c.srdK, 1, L_K + SLOT, vk0, vk1, vk2, vk3);
    barrier_vm<8>();
    pj.template piece<PFP>(c, 3, 2 * SLOT, dh1);
    barrier_vm<4>();
  }
  issue_kv(c, c.srdK, 2, L_K + 2 * SLOT, vk0, vk1, vk2, vk3);
  bf16x8 kf[16];
  #pragma unroll
  for (int i = 0; i < 16; ++i) kf[i] = lds_b128(L, L_K + (kA ^ (64u * (i & 3))) + 4096u * (i >> 2));
  barrier_vm<4>();
  bf16x8 qf[NQ][4];
  if (NQ == 1) {
    #pragma unroll
    for (int i = 0; i < 16; ++i) park[i] = bf16x8{};
  }
  bf16x8 ones = __builtin_bit_cast(bf16x8, u32x4{0x3F803F80u, 0x3F803F80u, 0x3F803F80u, 0x3F803F80u});
  PIN(ones);
  u32 s3 = 0;
  f32x4 O[8][NQ], negm[NQ], l[NQ];
  float m[NQ];
  #pragma unroll
  for (int i = 0; i < NQ; ++i) {
    m[i] = 0.f; negm[i] = f32x4{}; l[i] = f32x4{1.f, 1.f, 1.f, 1.f};
    #pragma unroll
    for (int d = 0; d < 8; ++d) O[d][i] = f32x4{};
  }
  auto step = [&](auto first_tag, const int rd, const int t) {
    constexpr bool FIRST = decltype(first_tag)::value;
    const u32 s3n = (s3 == 2) ? 0 : s3 + 1;
    const u32 vbase = L_V + s3 * SLOT, kbase = L_K + s3n * SLOT;
    const u32 sK = 8u * rd + t + 3;
    const bool kdma = FIRST || sK < 32;
    const u32 kdst = L_K + s3 * SLOT;
    float inv[NQ];
    if (FIRST) {
      #pragma unroll
      for (int i = 0; i < NQ; ++i)
        #pragma unroll
        for (int ks = 0; ks < 4; ++ks) qf[i][ks] = lds_b128(L, (qA ^ (64u * ks)) + 4096u * i);
      #pragma unroll
      for (int i = 0; i < NQ; ++i) inv[i] = __builtin_amdgcn_rcpf(l[i][0]);
      if (NQ == 1 && rd == 3) {
        if (ln < 8) { *lp<u64>(L_FLAG + 8u * ln) = d0; st_agent((char*)p.ws + WS_ACK + 128u * c.bid + 8u * ln, d0 ^ ACK_MIX); }
      }
    }
    if (NQ == 1 && rd < 3) {
      const u32 s1 = 8u * rd + t + 1;
      const int l2 = lane_id();
      const u32 voe = 8192u * (l2 >> 3) + 16u * ((l2 & 7) ^ (2u * ((l2 >> 4) & 1)));
      const __amdgpu_buffer_rsrc_t srdW = mksrd(p.wq[c.br], DM * NH * DH * 2);
      issue_wq_quarter(c, srdW, voe, (1 + (s1 >> 3)) & 3, s1 & 7, L_W + ((s1 + 1) & 1) * SLOT, 0);
      issue_wq_quarter(c, srdW, voe, (1 + (s1 >> 3)) & 3, s1 & 7, L_W + ((s1 + 1) & 1) * SLOT, 1);
    }
    if (!FIRST && sK == 34) { const int l2 = lane_id(); issue_wo(p, c, l2, 0, 0); issue_wo(p, c, l2, 1, 1); }
    if (!FIRST && NQ == 1 && rd == 1 && t == 1) {
      #pragma unroll
      for (int i = 0; i < 16; ++i) park[i] = lds_b128(L, ((L_QH + 256u * cl + 16u * (g ^ cl)) ^ (64u * (i & 3))) + 4096u * ((i >> 2) == 3 ? 6 : (i >> 2)));
    }
    u32 va[8];
    va[0] = vbase + vA;
    #pragma unroll
    for (int d = 1; d < 8; ++d) va[d] = va[0] ^ (32u * d);
    auto vread = [&](const int d, const int u) { return lds_tr2(L, va[d] + 8192u * u, va[d] + 8192u * u + 4096u); };
    if (FIRST) { waitlgkm<0>(); SB(); }
    f32x4 S[4][NQ];
    bf16x8 vf[16];
    #pragma unroll
    for (int kt = 0; kt < 2; ++kt)
      #pragma unroll
      for (int ks = 0; ks < 4; ++ks) {
        #pragma unroll
        for (int i = 0; i < NQ; ++i) { if (ks == 0) MFMA_INIT(S[kt][i], kf[4 * kt + ks], qf[i][ks], negm[i]); else MFMA_ACC(S[kt][i], kf[4 * kt + ks], qf[i][ks]); }
        vf[4 * kt + ks] = vread(4 * kt + ks, 0);
        if (FIRST) {
          const int d = 4 * kt + ks;
          #pragma unroll
          for (int i = 0; i < NQ; ++i) lds_w64(L, rowq + 4096u * i + (oW ^ (32u * d)), pack4(O[d][i], inv[i]));
        }
        SB();
      }
    u32x4 pw[NQ][2];
    float pa[NQ];
    #pragma unroll
    for (int kt = 2; kt < 4; ++kt)
      #pragma unroll
      for (int ks = 0; ks < 4; ++ks) {
        const int n = 4 * (kt - 2) + ks;
        #pragma unroll
        for (int i = 0; i < NQ; ++i) { if (ks == 0) MFMA_INIT(S[kt][i], kf[4 * kt + ks], qf[i][ks], negm[i]); else MFMA_ACC(S[kt][i], kf[4 * kt + ks], qf[i][ks]); }
        vf[8 + n] = vread(n, 1);
        if (n >= 2 && n < 6) {
          const int k2 = (n - 2) >> 1, e = 2 * ((n - 2) & 1);
          #pragma unroll
          for (int i = 0; i < NQ; ++i) {
            const float x0 = S[k2][i][e], x1 = S[k2][i][e + 1];
            pa[i] = (n == 2) ? __builtin_elementwise_maximum(x0, x1) : vmax3(pa[i], x0, x1);
            u32 pk = cvtpk(EXP2(x0), EXP2(x1)); PIN(pk);
            pw[i][0][2 * k2 + (e >> 1)] = pk;
          }
        }
        SB();
      }
    asm volatile("" ::: "memory");
    NOP8();
    SB();
    #pragma unroll
    for (int i = 0; i < NQ; ++i) {
      const float pb = vmax3(S[2][i][0], S[2][i][1], S[2][i][2]);
      pa[i] = vmax3(pa[i], S[2][i][3], S[3][i][0]); pa[i] = vmax3(pa[i], S[3][i][1], S[3][i][2]);
      pa[i] = vmax3(pa[i], pb, S[3][i][3]);
    }
    bool move;
    if (FIRST) {
      move = false;
      #pragma unroll
      for (int i = 0; i < NQ; ++i) { pa[i] = xg_max(pa[i]); move |= !(__builtin_fabsf(pa[i]) <= THR); }
    } else move = (NQ == 2 ? __builtin_elementwise_maximum(pa[0], pa[NQ - 1]) : pa[0]) > THR;
    if (__builtin_expect(__any(move), 0)) {
      #pragma unroll
      for (int i = 0; i < NQ; ++i) {
        const float delta = FIRST ? pa[i] : fmaxf(xg_max(pa[i]), 0.f);
        m[i] += delta;
        negm[i] = f32x4{-m[i], -m[i], -m[i], -m[i]};
        #pragma unroll
        for (int kt = 0; kt < 4; ++kt) S[kt][i] -= delta;
        #pragma unroll
        for (int k2 = 0; k2 < 2; ++k2)
          #pragma unroll
          for (int e = 0; e < 4; e += 2) pw[i][0][2 * k2 + (e >> 1)] = cvtpk(EXP2(S[k2][i][e]), EXP2(S[k2][i][e + 1]));
        if (!FIRST) {
          const float alpha = EXP2(-delta);
          l[i] *= alpha;
          #pragma unroll
          for (int d = 0; d < 8; ++d) O[d][i] *= alpha;
        }
      }
      SB();
      NOP2();
    }
    if (kdma) issue_kv(c, c.srdK, sK, kdst, vk0, vk1, vk2, vk3);
    waitlgkm<15>();
    SB();
    #pragma unroll
    for (int i = 0; i < NQ; ++i) { if (FIRST) MFMA_ZERO(l[i], ones, __builtin_bit_cast(bf16x8, pw[i][0])); else MFMA_ACC(l[i], ones, __builtin_bit_cast(bf16x8, pw[i][0])); }
    #pragma unroll
    for (int d = 0; d < 8; ++d) {
      #pragma unroll
      for (int i = 0; i < NQ; ++i) { if (FIRST) MFMA_ZERO(O[d][i], vf[d], __builtin_bit_cast(bf16x8, pw[i][0])); else MFMA_ACC(O[d][i], vf[d], __builtin_bit_cast(bf16x8, pw[i][0])); }
      if (d < 4) {
        const int k2 = 2 + (d >> 1), e = 2 * (d & 1);
        #pragma unroll
        for (int i = 0; i < NQ; ++i) { u32 pk = cvtpk(EXP2(S[k2][i][e]), EXP2(S[k2][i][e + 1])); PIN(pk); pw[i][1][2 * (k2 - 2) + (e >> 1)] = pk; }
      }
      SB();
    }
    u32 ka[4];
    ka[0] = kbase + kA;
    #pragma unroll
    for (int ks = 1; ks < 4; ++ks) ka[ks] = ka[0] ^ (64u * ks);
    waitlgkm<0>();
    SB();
    #pragma unroll
    for (int i = 0; i < NQ; ++i) MFMA_ACC(l[i], ones, __builtin_bit_cast(bf16x8, pw[i][1]));
    #pragma unroll
    for (int d = 0; d < 8; ++d) {
      #pragma unroll
      for (int i = 0; i < NQ; ++i) MFMA_ACC(O[d][i], vf[8 + d], __builtin_bit_cast(bf16x8, pw[i][1]));
      SB();
      kf[2 * d] = lds_b128(L, ka[(2 * d) & 3] + 4096u * ((2 * d) >> 2));
      kf[2 * d + 1] = lds_b128(L, ka[(2 * d + 1) & 3] + 4096u * ((2 * d + 1) >> 2));
      SB();
    }
    s3 = s3n;
    if (FIRST && NQ == 1 && rd == 3) barrier_vm<5>();
    else if (kdma) barrier_vm<4>(); else barrier_vm<0>();
  };
  #pragma unroll 1
  for (int rd = 0; rd < 4; ++rd) {
    step(std::true_type{}, rd, 0);
    #pragma unroll 1
    for (int t = 1; t < 8; ++t) step(std::false_type{}, rd, t);
  }
  NOP8();
  SB();
  #pragma unroll
  for (int i = 0; i < NQ; ++i) {
    const float inv = __builtin_amdgcn_rcpf(l[i][0]);
    #pragma unroll
    for (int d = 0; d < 8; ++d) lds_w64(L, rowq - L_QH + 4096u * i + (oW ^ (32u * d)), pack4(O[d][i], inv));
  }
}

template <int NQ>
DI void projection_role(const Params& p, Ctx& c, bf16x8 (&park)[16]) {
  LAS char* L = c.L;
  const u32 w = c.w, wave = c.wave; (void)wave;
  const u32 b = c.b, j = c.j; (void)j;
  const int ln = lane_id();
  const u32 cl = ln & 15, g = ln >> 4, q4 = cl >> 2, p4 = ln & 3;
  const u32 vv0 = 256u * g + 16u * (cl ^ (2u * g)), vv2 = vv0 ^ 128u;
  const __amdgpu_buffer_rsrc_t srdW = mksrd(p.wq[c.br], DM * NH * DH * 2);
  const u32 rot = KROT, dh0 = DROT;
  const u32 vo_w = wq_pattern(c, ln);
  auto issue_w = [&](u32 rdn, u32 pi, u32 la) { issue_wq(c, srdW, vo_w, rdn, pi, la); };
  Proj<NQ> pj;
  pj.init(c, ln);
  const u32 rowq = pj.rowq;
  const u32 qA = rowq + 16u * (g ^ cl);
  issue_w(0, 0, 7 * SLOT); issue_x_rows(p, c, ln); issue_w(0, 1, 9 * SLOT);
  barrier_vm<4>();
  pj.read_x(c, ln);
  barrier_lgkm();
  issue_w(0, 2, 5 * SLOT);
  pj.template piece<PFP>(c, 0, 7 * SLOT, dh0);
  barrier_vm<4>();
  issue_w(0, 3, 6 * SLOT);
  pj.template piece<PFP>(c, 1, 9 * SLOT, dh0);
  barrier_vm<4>();
  issue_w(1, 0, 7 * SLOT);
  pj.template piece<PFP>(c, 2, 5 * SLOT, dh0);
  issue_kv(c, c.srdV, 0, L_V, vv0, vv0, vv2, vv2);
  barrier_vm<8>();
  issue_kv(c, c.srdV, 1, L_V + SLOT, vv0, vv0, vv2, vv2);
  pj.template piece<PFP>(c, 3, 6 * SLOT, dh0);
  barrier_lgkm();
  barrier_vm<4>();
  bf16x8 og1[NQ][4];
  #pragma unroll
  for (int i = 0; i < NQ; ++i)
    #pragma unroll
    for (int ks = 0; ks < 4; ++ks) og1[i][ks] = bf16x8{};
  if (NQ == 1) {
    #pragma unroll
    for (int i = 4; i < 16; ++i) park[i] = bf16x8{};
  }
  u32 s3 = 0;
  #pragma unroll 1
  for (int hh = 0; hh < 6; ++hh) {
    #pragma unroll
    for (int kq = 0; kq < 4; ++kq) {
      const u32 s = 4u * hh + kq, s1 = s + 1;
      const u32 vdst = L_V + ((s3 == 0) ? 2 : s3 - 1) * SLOT;
      if (NQ == 1) {
        issue_wq_quarter(c, srdW, vo_w ^ 64u, (1 + (s1 >> 3)) & 3, s1 & 7, L_W + (kq & 1) * SLOT, 2);
        issue_wq_quarter(c, srdW, vo_w ^ 64u, (1 + (s1 >> 3)) & 3, s1 & 7, L_W + (kq & 1) * SLOT, 3);
      }
      if (NQ == 2) issue_kv(c, c.srdV, s + 2, vdst, vv0, vv0, vv2, vv2);
      if (NQ == 1 && kq == 1 && hh == 2) {
        #pragma unroll
        for (int i = 4; i < 16; ++i) park[i] = lds_b128(L, ((L_QH + 256u * cl + 16u * (g ^ cl)) ^ (64u * (i & 3))) + 4096u * (2 + (i >> 2)));
      }
      if (kq == 1 && hh == 4) {
        #pragma unroll
        for (int i = 0; i < NQ; ++i)
          #pragma unroll
          for (int ks = 0; ks < 4; ++ks) og1[i][ks] = lds_b128(L, (qA ^ (64u * ks)) + 4096u * i);
      }
      pj.piece(c, kq, L_W + ((kq + 1) & 1) * SLOT, (u32)(hh & 1) ^ dh0);
      if (NQ == 1) issue_kv(c, c.srdV, s + 2, vdst, vv0, vv0, vv2, vv2);
      s3 = (s3 == 2) ? 0 : s3 + 1;
      barrier_vm<4>();
    }
  }
  #pragma unroll
  for (int i = 0; i < 4 * NQ; ++i) park[i] = bf16x8{};
  const __amdgpu_buffer_rsrc_t srdO = mksrd(p.wo[c.br], NH * DH * DM * 2);
  #pragma unroll 1
  for (u32 s = 24; s < 32; ++s) {
    const u32 vdst = L_V + ((s3 == 0) ? 2 : s3 - 1) * SLOT;
    if (s + 2 < 32) issue_kv(c, c.srdV, s + 2, vdst, vv0, vv0, vv2, vv2);
    if (s == WARMS) {
      #pragma unroll
      for (int i = 0; i < 8; ++i) dma(rfl(c.ldsb + L_W + 1024u * w + 4096u * i), 16u * ln, srdO, rfl((c.hg * 512u + 32u * b + 4u * i + w) * 1024u));
    }
    if (s == 31) { issue_wo(p, c, ln, 0, 0); issue_wo(p, c, ln, 1, 1); }
    if (s == 25) {
      #pragma unroll
      for (int i = 0; i < NQ; ++i)
        #pragma unroll
        for (int ks = 0; ks < 4; ++ks) park[4 * i + ks] = lds_b128(L, (qA ^ (64u * ks)) + 4096u * i);
    }
    s3 = (s3 == 2) ? 0 : s3 + 1;
    if (s >= 30) barrier_vm<0>(); else if (s == WARMS || s == WARMS + 1) barrier_vm<12>(); else barrier_vm<4>();
  }
  #pragma unroll
  for (int i = 0; i < NQ; ++i)
    #pragma unroll
    for (int ks = 0; ks < 4; ++ks) lds_w128(L, OREG + ((qA - L_QH) ^ (64u * ks)) + 4096u * i, og1[i][ks]);
}

__global__ __launch_bounds__(512, 2) void fused_attn(Params p) {
  extern __shared__ __attribute__((aligned(16))) char smem[];
  Ctx c;
  c.L = (LAS char*)smem;
  c.ldsb = (u32)(size_t)smem;
  c.wave = rfl(threadIdx.x >> 6);
  c.w = c.wave & 3;
  const u32 wave = c.wave, w = c.w;
  const u32 bid = blockIdx.x, xcd = bid & 7;
  c.j = bid >> 3; c.bid = bid;
  c.br = xcd & 1; c.hg = 2 * (xcd >> 1) + (c.j & 1); c.b = c.j >> 1;
  const size_t kvoff = (size_t)((c.b * NH + c.hg * 4) * SK) * DH * 2;
  c.srdK = mksrd((const char*)p.k[c.br] + kvoff, 4 * SK * DH * 2);
  c.srdV = mksrd((const char*)p.v[c.br] + kvoff, 4 * SK * DH * 2);
  bf16x8 park[16];
  if (wave < 3) attention_role<2>(p, c, park);
  else if (wave == 3) attention_role<1>(p, c, park);
  else if (wave < 7) projection_role<2>(p, c, park);
  else projection_role<1>(p, c, park);

  {
    LAS char* L = c.L;
    const int ln = lane_id();
    const u32 cl = ln & 15, g = ln >> 4, q4 = cl >> 2, p4 = ln & 3;
    const char* ackp = (const char*)p.ws + WS_ACK + 128u * member_bid(c, ln & 7) + 8u * c.hg;
    u64 ack;
    asm volatile("global_load_dwordx2 %0, %1, off sc1" : "=&v"(ack) : "v"(ackp) : "memory");
    issue_wo(p, c, ln, 2, 2);
    barrier_lgkm();
    const u32 oA = 256u * cl + 16u * (g ^ cl);
    const u32 wA = 128u * (8u * g + q4) + 16u * ((2u * ((q4 >> 1) + 2u * (g & 1))) | (p4 >> 1)) + 8u * (p4 & 1);
    auto aread = [&](bf16x8 (&af)[4], u32 slot3) {
      u32 rb = rfl(ring_slot(wave, slot3));
      OPAQUE_S(rb);
      const u32 a0 = rb + wA;
      #pragma unroll
      for (int nt = 0; nt < 4; ++nt) { const u32 a = a0 ^ (32u * nt); af[nt] = lds_tr2(L, a, a + 512u); }
    };
    f32x4 acc[4][7];
    bf16x8 afA[4], afB[4], bq[7];
    auto oaddr = [&](u32 u) { return OREG * ((u >> 2) & 1) + (oA ^ (64u * (u & 3))); };
    SB();
    aread(afA, 0);
    {
      const u32 ob = oaddr(0);
      #pragma unroll
      for (int qt = 0; qt < 4; ++qt) bq[qt] = lds_b128(L, ob + 4096u * qt);
    }
    u32 sl = 0;
    auto kstep = [&](const u32 u, bf16x8 (&af)[4], bf16x8 (&afn)[4]) {
      const u32 sl1 = (sl == 2) ? 0 : sl + 1;
      const u32 ob = oaddr(u), obn = oaddr(u + 1);
      const bool pre = (u & 7) != 7;
      #pragma unroll
      for (int qt = 0; qt < 7; ++qt) {
        #pragma unroll
        for (int nt = 0; nt < 4; ++nt) { if (u == 0) MFMA_ZERO(acc[nt][qt], af[nt], bq[qt]); else MFMA_ACC(acc[nt][qt], af[nt], bq[qt]); }
        SB();
        if (qt < 3) bq[qt + 4] = lds_b128(L, ob + 4096u * (qt + 4));
        else if (pre) bq[qt - 3] = lds_b128(L, obn + 4096u * (qt - 3));
        if (qt == 0) issue_wo(p, c, ln, u + 3, sl);
        if (qt == 2) { waitvm<8>(); if (u == 0) asm volatile("" : "+v"(ack)); aread(afn, sl1); }
        SB();
      }
      sl = sl1;
    };
    #pragma unroll
    for (u32 u = 0; u < 16; u += 2) {
      if (u == 8) {
        barrier_lgkm();
        if (wave == 3 || wave == 7) {
          #pragma unroll
          for (int i = 0; i < 16; ++i) {
            if (wave == 3) lds_w128(L, (oA ^ (64u * (i & 3))) + 4096u * ((i >> 2) == 3 ? 6 : (i >> 2)), park[i]);
            else if (i >= 4) lds_w128(L, (oA ^ (64u * (i & 3))) + 4096u * (2 + (i >> 2)), park[i]);
          }
        }
        if (wave >= 4) {
          #pragma unroll
          for (int ks = 0; ks < 4; ++ks) {
            lds_w128(L, OREG + (oA ^ (64u * ks)) + 8192u * w, park[ks]);
            if (w < 3) lds_w128(L, OREG + (oA ^ (64u * ks)) + 8192u * w + 4096u, park[4 + ks]);
          }
        }
        barrier_lgkm();
        const u32 ob = oaddr(8);
        #pragma unroll
        for (int qt = 0; qt < 4; ++qt) bq[qt] = lds_b128(L, ob + 4096u * qt);
      }
      kstep(u, afA, afB);
      kstep(u + 1, afB, afA);
    }
    waitvm<0>();
    NOP8();
    SB();
    const __amdgpu_buffer_rsrc_t srdP = mksrd((const char*)p.ws + WS_P + (size_t)(c.br * NB + c.b) * 8 * PART_WG, 8 * PART_WG);
    #pragma unroll
    for (int qt = 0; qt < 7; ++qt)
      #pragma unroll
      for (int np = 0; np < 2; ++np) {
        const u32x2 a = pack4n(acc[2 * np][qt]), bb = pack4n(acc[2 * np + 1][qt]);
        __builtin_amdgcn_raw_buffer_store_b128(u32x4{a[0], a[1], bb[0], bb[1]}, srdP, c.hg * PART_WG + wave * 14336u + (qt * 2 + np) * 1024u + ln * 16u, 0, 16);
      }
    waitvm<0>();
    barrier_lgkm();
    if (wave == 0) {
      const u64 e0 = *lp<u64>(L_FLAG + 8u * (ln & 7));
      const u64 want = *lp<u64>(L_FLAG + 8u * c.hg) ^ ACK_MIX;
      for (u32 spin = 0; !__all(ack == want) && spin < SPIN_CAP; ++spin) { __builtin_amdgcn_s_sleep(8); ack = ld_agent(ackp); }
      if (ln == (int)c.hg) st_agent((char*)p.ws + WS_DONE + 128u * c.bid, e0 + 1);
      const char* dp = (const char*)p.ws + WS_DONE + 128u * member_bid(c, ln & 7);
      for (u32 spin = 0; spin < SPIN_CAP; ++spin) { if (__all(ld_agent(dp) == e0 + 1)) break; __builtin_amdgcn_s_sleep(2); }
    }
    barrier_lgkm();
    if (wave < 7) {
      u32x4 x[2][8];
      #pragma unroll
      for (int np = 0; np < 2; ++np)
        #pragma unroll
        for (int m = 0; m < 8; ++m) x[np][m] = __builtin_amdgcn_raw_buffer_load_b128(srdP, m * PART_WG + c.hg * 14336u + (wave * 2 + np) * 1024u + ln * 16u, 0, 16);
      const u32 tile = ring_slot(wave, 0);
      #pragma unroll
      for (int np = 0; np < 2; ++np) {
        float s8[8] = {};
        #pragma unroll
        for (int m = 0; m < 8; ++m)
          #pragma unroll
          for (int i = 0; i < 4; ++i) { s8[2 * i] += __uint_as_float(x[np][m][i] << 16); s8[2 * i + 1] += __uint_as_float(x[np][m][i] & 0xffff0000u); }
        lds_w64(L, tile + cl * 128u + 64u * np + 8u * g, u32x2{cvtpk(s8[0], s8[1]), cvtpk(s8[2], s8[3])});
        lds_w64(L, tile + cl * 128u + 64u * np + 32u + 8u * g, u32x2{cvtpk(s8[4], s8[5]), cvtpk(s8[6], s8[7])});
      }
      #pragma unroll
      for (int i = 0; i < 2; ++i) {
        const u32 row = 8u * i + (ln >> 3), ch = ln & 7;
        char* o = (char*)p.out + (((size_t)c.b * SQ + 16u * wave + row) * (2 * DM) + c.br * DM + 64u * c.hg) * 2 + 16u * ch;
        *(bf16x8*)o = lds_b128(L, tile + row * 128u + 16u * ch);
      }
    }
  }
}

extern "C" void kernel_launch(void* const* d_in, const int* in_sizes, int n_in, void* d_out, int out_size, void* d_ws, size_t ws_size,
                              hipStream_t stream) {
  Params p;
  for (int i = 0; i < 2; ++i) { p.x[i] = d_in[5 * i]; p.k[i] = d_in[5 * i + 1]; p.v[i] = d_in[5 * i + 2]; p.wq[i] = d_in[5 * i + 3]; p.wo[i] = d_in[5 * i + 4]; }
  p.out = d_out; p.ws = d_ws;
  (void)hipFuncSetAttribute((const void*)fused_attn, hipFuncAttributeMaxDynamicSharedMemorySize, LDS_BYTES);
  fused_attn<<<256, 512, LDS_BYTES, stream>>>(p);
}
```
